```python
import torch

def _softmax(x, axis=-1):
    return torch.softmax(x, dim=axis)
def _gelu(x):
    return torch.nn.functional.gelu(x, approximate="tanh")
def _silu(x):
    return torch.nn.functional.silu(x)
def _sigmoid(x):
    return torch.sigmoid(x)
def _layernorm(x, axis=-1, eps=1e-5):
    m = x.mean(dim=axis, keepdim=True)
    v = x.var(dim=axis, keepdim=True, unbiased=False)
    return (x - m) / torch.sqrt(v + eps)
def _scatter_add(tgt, idx, src, axis=0):
    return tgt.clone().index_add_(axis, idx, src)
def _segment_sum(src, seg, n):
    out = torch.zeros((n,) + src.shape[1:], dtype=src.dtype)
    return out.index_add_(0, seg, src)
def _pad(x, pw):
    flat = tuple(v for pair in reversed(pw) for v in pair)
    return torch.nn.functional.pad(x, flat)

kernel_name = 'gated_mix__ssm_conv_s4__glu_gated_glu__prompt'

def setup_inputs():

    _w = (lambda a: (lambda t: t.to(torch.bfloat16) if t.is_floating_point() else t)(torch.as_tensor(a)))
    def _A_setup():
        B, L, D, N = 1, 2048, 512, 128
        A_re = -(lambda _x: torch.exp(torch.as_tensor(_x)))((lambda *s: torch.randn(*s, dtype=torch.bfloat16) * 6.4)(D, N))
        A_im = torch.pi * (lambda *s: torch.randn(*s, dtype=torch.bfloat16) * 0.1)(D, N)
        B_re = (lambda *s: torch.randn(*s, dtype=torch.bfloat16) * 0.2)(D, N)
        B_im = (lambda *s: torch.randn(*s, dtype=torch.bfloat16) * 0.2)(D, N)
        C_re = (lambda *s: torch.randn(*s, dtype=torch.bfloat16) * 0.2)(D, N)
        C_im = (lambda *s: torch.randn(*s, dtype=torch.bfloat16) * 0.2)(D, N)
        log_dt = (lambda *s: torch.randn(*s, dtype=torch.bfloat16) * 0.1)(D) * 0.5 - 4.0
        Dskip = (lambda *s: torch.randn(*s, dtype=torch.bfloat16) * 0.1)(D)
        return {
            "u": _w((lambda *s: torch.randn(*s, dtype=torch.bfloat16) * 0.05)(B, L, D)),
            "A_re": _w(A_re),
            "A_im": _w(A_im),
            "B_re": _w(B_re),
            "B_im": _w(B_im),
            "C_re": _w(C_re),
            "C_im": _w(C_im),
            "log_dt": _w(log_dt),
            "Dskip": _w(Dskip),
        }
    _A_in = _A_setup()

    def _B_setup():
        B, S, D, F = 1, 2048, 512, 1792
        return {
            "x": _w((lambda *s: torch.randn(*s, dtype=torch.bfloat16) * 0.05)(B, S, D)),
            "w_gate": _w((lambda *s: torch.randn(*s, dtype=torch.bfloat16) * 0.4)(D, F)),
            "w_up": _w((lambda *s: torch.randn(*s, dtype=torch.bfloat16) * 0.05)(D, F)),
            "w_down": _w((lambda *s: torch.randn(*s, dtype=torch.bfloat16) * 0.05)(F, D)),
        }
    _B_in = _B_setup()

    return {
        "A_u": _A_in["u"],
        "A_A_re": _A_in["A_re"],
        "A_A_im": _A_in["A_im"],
        "A_B_re": _A_in["B_re"],
        "A_B_im": _A_in["B_im"],
        "A_C_re": _A_in["C_re"],
        "A_C_im": _A_in["C_im"],
        "A_log_dt": _A_in["log_dt"],
        "A_Dskip": _A_in["Dskip"],
        "B_x": _B_in["x"],
        "B_w_gate": _B_in["w_gate"],
        "B_w_up": _B_in["w_up"],
        "B_w_down": _B_in["w_down"],
        "gate_w": _w((lambda *s: torch.randn(*s, dtype=torch.bfloat16) * 0.05)(512) * 16.0),
    }

def reference(A_u, A_A_re, A_A_im, A_B_re, A_B_im, A_C_re, A_C_im, A_log_dt, A_Dskip, B_x, B_w_gate, B_w_up, B_w_down, gate_w):
    def _A_ref(u, A_re, A_im, B_re, B_im, C_re, C_im, log_dt, Dskip):
        Bsz, L, D = u.shape
        dt = torch.exp(log_dt)[:, None]
        A = A_re + 1j * A_im
        Bc = B_re + 1j * B_im
        Cc = C_re + 1j * C_im
        logdA = dt * A
        dB = dt * Bc
        l = (lambda x, dt: x.to(dt))(torch.arange(L), A_re.dtype)
        P = torch.exp(logdA[:, :, None] * l[None, None, :])
        K = torch.einsum("dn,dnl,dn->dl", Cc, P, dB).real
        L2 = 2 * L
        Kp = _pad(K, ((0, 0), (0, L)))
        up = (lambda x, *d: x.permute(*d))(u, 0, 2, 1)
        up = _pad(up, ((0, 0), (0, 0), (0, L)))
        Kf = (lambda x, axes=(-2,-1): torch.fft.rfft2(x.float(), dim=axes))(Kp[:, None, :], axes=(-2, -1))
        Uf = (lambda x, axes=(-2,-1): torch.fft.rfft2(x.float(), dim=axes))(up[:, :, None, :], axes=(-2, -1))
        Yf = Uf * Kf[None, :, :, :]
        y = (lambda x, s, axes=(-2,-1): torch.fft.irfft2(x, s=s, dim=axes).to(torch.bfloat16))(Yf, (1, L2), axes=(-2, -1))[:, :, 0, :L]
        y = (lambda x, *d: x.permute(*d))(y, 0, 2, 1) + u * Dskip[None, None, :]
        return y

    def _B_ref(x, w_gate, w_up, w_down):
        g = torch.einsum("bsd,df->bsf", x, w_gate)
        u = torch.einsum("bsd,df->bsf", x, w_up)
        h = _sigmoid(g) * u
        return torch.einsum("bsf,fd->bsd", h, w_down)

    x = A_u
    g = _sigmoid((x * gate_w).sum(-1, keepdims=True))
    a = _A_ref(x, A_A_re, A_A_im, A_B_re, A_B_im, A_C_re, A_C_im, A_log_dt, A_Dskip)
    b = _B_ref(x, B_w_gate, B_w_up, B_w_down)
    return g * a + (1 - g) * b
```

```cpp
#include <hip/hip_runtime.h>
#include <hip/hip_bf16.h>
#include <stdint.h>

typedef unsigned short u16;
__device__ __forceinline__ float bf2f(u16 v) { return __uint_as_float(((unsigned)v) << 16); }
__device__ __forceinline__ u16 f2bf(float f) { __hip_bfloat16 b = __float2bfloat16(f); return *(u16*)&b; }
__device__ __forceinline__ float rbf(float f) { return bf2f(f2bf(f)); }

constexpr int L = 2048, D = 512, N = 128, F = 1792;

__global__ void k_gate(const u16* x, const u16* gw, float* g) {
  int t = blockIdx.x; int lane = threadIdx.x;
  float s = 0.f;
  for (int d = lane; d < D; d += 64) s += rbf(bf2f(x[t * D + d]) * bf2f(gw[d]));
  for (int o = 32; o > 0; o >>= 1) s += __shfl_xor(s, o);
  if (lane == 0) { float z = rbf(s); g[t] = rbf(1.f / (1.f + expf(-z))); }
}

__global__ void k_kgen(const u16* Are, const u16* Aim, const u16* Bre, const u16* Bim, const u16* Cre, const u16* Cim,
                       const u16* logdt, float* K) {
  int d = blockIdx.y; int s = blockIdx.x * 256 + threadIdx.x;
  float dt = rbf(expf(bf2f(logdt[d])));
  float l = rbf((float)s);
  float acc = 0.f;
  for (int n = 0; n < N; ++n) {
    float ar = dt * bf2f(Are[d * N + n]), ai = dt * bf2f(Aim[d * N + n]);
    float br = dt * bf2f(Bre[d * N + n]), bi = dt * bf2f(Bim[d * N + n]);
    float cr = bf2f(Cre[d * N + n]), ci = bf2f(Cim[d * N + n]);
    float m = expf(ar * l); float sn, cs; sincosf(ai * l, &sn, &cs);
    float pr = m * cs, pi = m * sn;
    float tr = cr * pr - ci * pi, ti = cr * pi + ci * pr;
    acc += tr * br - ti * bi;
  }
  K[d * L + s] = acc;
}

__global__ void k_conv(const u16* x, const float* K, const u16* Dskip, u16* a) {
  int d = blockIdx.y; int t = blockIdx.x * 256 + threadIdx.x;
  float acc = 0.f;
  for (int s = 0; s <= t; ++s) acc += K[d * L + s] * bf2f(x[(t - s) * D + d]);
  float y = rbf(acc);
  float sk = rbf(bf2f(x[t * D + d]) * bf2f(Dskip[d]));
  a[t * D + d] = f2bf(y + sk);
}

__global__ void k_gemm1(const u16* x, const u16* wg, const u16* wu, u16* H) {
  int f = blockIdx.x * 256 + threadIdx.x; int t = blockIdx.y;
  float g = 0.f, u = 0.f;
  for (int k = 0; k < D; ++k) { float xv = bf2f(x[t * D + k]); g += xv * bf2f(wg[k * F + f]); u += xv * bf2f(wu[k * F + f]); }
  g = rbf(g); u = rbf(u);
  float sg = rbf(1.f / (1.f + expf(-g)));
  H[t * F + f] = f2bf(sg * u);
}

__global__ void k_gemm2(const u16* H, const u16* wd, const u16* a, const float* g, u16* out) {
  int d = blockIdx.x * 256 + threadIdx.x; int t = blockIdx.y;
  float b = 0.f;
  for (int k = 0; k < F; ++k) b += bf2f(H[t * F + k]) * bf2f(wd[k * D + d]);
  b = rbf(b);
  float gg = g[t];
  float t1 = rbf(gg * bf2f(a[t * D + d]));
  float t2 = rbf(rbf(1.f - gg) * b);
  out[t * D + d] = f2bf(t1 + t2);
}

extern "C" void kernel_launch(void* const* d_in, const int* in_sizes, int n_in,
                              void* d_out, int out_size, void* d_ws, size_t ws_size,
                              hipStream_t stream) {
  const u16* x = (const u16*)d_in[0];
  const u16 *Are = (const u16*)d_in[1], *Aim = (const u16*)d_in[2], *Bre = (const u16*)d_in[3], *Bim = (const u16*)d_in[4];
  const u16 *Cre = (const u16*)d_in[5], *Cim = (const u16*)d_in[6], *logdt = (const u16*)d_in[7], *Dskip = (const u16*)d_in[8];
  const u16 *wg = (const u16*)d_in[10], *wu = (const u16*)d_in[11], *wd = (const u16*)d_in[12], *gw = (const u16*)d_in[13];
  char* ws = (char*)d_ws;
  float* g = (float*)ws; ws += L * 4;
  float* K = (float*)ws; ws += (size_t)D * L * 4;
  u16* a = (u16*)ws; ws += (size_t)L * D * 2;
  u16* H = (u16*)ws;
  k_gate<<<L, 64, 0, stream>>>(x, gw, g);
  k_kgen<<<dim3(L / 256, D), 256, 0, stream>>>(Are, Aim, Bre, Bim, Cre, Cim, logdt, K);
  k_conv<<<dim3(L / 256, D), 256, 0, stream>>>(x, K, Dskip, a);
  k_gemm1<<<dim3(F / 256, L), 256, 0, stream>>>(x, wg, wu, H);
  k_gemm2<<<dim3(D / 256, L), 256, 0, stream>>>(H, wd, a, g, (u16*)d_out);
}
```

```cpp
#include <hip/hip_runtime.h>
#include <stdint.h>

typedef unsigned short u16;
typedef unsigned long long u64;
#define LAS __attribute__((address_space(3)))
#define GAS __attribute__((address_space(1)))
#define DI __device__ __forceinline__
typedef LAS char lchar;
typedef __attribute__((ext_vector_type(8))) short bf16x8;
typedef __attribute__((ext_vector_type(4))) short s16x4;
typedef __attribute__((ext_vector_type(4))) float f32x4;
typedef __attribute__((ext_vector_type(2))) float f32x2;
typedef __attribute__((ext_vector_type(16))) float f32x16;
typedef __attribute__((ext_vector_type(2))) unsigned u32x2;
typedef __attribute__((ext_vector_type(4))) unsigned u32x4;
typedef __attribute__((ext_vector_type(2))) __bf16 bf16x2_t;
typedef __amdgpu_buffer_rsrc_t rsrc_t;

constexpr int L = 2048, D = 512, NS = 128, F = 1792;
constexpr unsigned WS_EPOCH = 0, WS_FX = 4096, WS_FW = 8192, WS_FG = 12288, WS_FH = 16384, WS_FA = 20480, WS_TMO = 24576;
constexpr unsigned WS_G = 32768, WS_XT = 65536, WS_WF = WS_XT + D * L * 2, WS_AT = WS_WF + D * F * 2, WS_HF = WS_AT + D * L * 2;
constexpr unsigned WS_DBG = WS_HF + L * F * 2;
constexpr int UPOFF = 15488, WREG = 30848, XREG = 16384, STAGE = WREG + XREG;
constexpr int L_EXTRA = 3 * STAGE;
constexpr int L_SYNC = L_EXTRA;
constexpr int L_SCR = L_SYNC + 256;
constexpr int KTAB = 4352, KROW = 272;
constexpr int L_END_A = L_SCR + 4 * KTAB;
static_assert(L_END_A <= 163840 - 2048 && 4 * KTAB >= 15360, "LDS");
constexpr int KCOPY = 4160;
constexpr int L_KC = 2 * STAGE;
constexpr int L_U = L_KC + 2 * 4 * KCOPY;
constexpr int UIMG = 5120;
static_assert(L_U + 2 * UIMG <= 3 * STAGE, "slot2");
constexpr int L_Y = 0;
constexpr int L_HAND = 2 * STAGE + WREG;
constexpr int L_RING = 0;
constexpr int L_AT = 131072;
constexpr int PSTR = 272, PKQ = 64 * PSTR;
constexpr int LDS_BYTES = 163840;

#define BAR() __builtin_amdgcn_s_barrier()
#define SB() __builtin_amdgcn_sched_barrier(0)
#define WAITV(n) asm volatile("s_waitcnt vmcnt(%0)" ::"n"(n) : "memory")
#define WAITL0() asm volatile("s_waitcnt lgkmcnt(0)" ::: "memory")
constexpr int SC1 = 16;

DI rsrc_t mk(const void* p, unsigned bytes) { return __builtin_amdgcn_make_buffer_rsrc((void*)p, 0, bytes, 0x00020000); }
template <int AUX = 0> DI void blds16(rsrc_t r, lchar* l, unsigned voff, unsigned soff) { __builtin_amdgcn_raw_ptr_buffer_load_lds(r, (LAS void*)l, 16, voff, soff, 0, AUX); }
DI s16x4 tr_read(lchar* p) { return __builtin_amdgcn_ds_read_tr16_b64_v4i16((LAS s16x4*)p); }
DI bf16x8 tr_read2_asm(lchar* p, int off2) {
  s16x4 a, b;
  asm volatile("ds_read_b64_tr_b16 %0, %2\n\tds_read_b64_tr_b16 %1, %2 offset:%3\n\ts_waitcnt lgkmcnt(0)" : "=&v"(a), "=&v"(b) : "v"((unsigned)(size_t)p), "i"(off2) : "memory");
  return __builtin_shufflevector(a, b, 0, 1, 2, 3, 4, 5, 6, 7);
}
DI bf16x8 lds_b128(lchar* p) { return *(const LAS bf16x8*)p; }
DI void st128(u32x4 v, rsrc_t r, unsigned off) { __builtin_amdgcn_raw_buffer_store_b128(v, r, off, 0, SC1); }
DI unsigned pk_bf16(float a, float b) { bf16x2_t v = {(__bf16)a, (__bf16)b}; return __builtin_bit_cast(unsigned, v); }
DI float bf_lo(unsigned v) { return __uint_as_float(v << 16); }
DI float bf_hi(unsigned v) { return __uint_as_float(v & 0xffff0000u); }
DI float bf2f(u16 v) { return __uint_as_float(((unsigned)v) << 16); }
DI float rbf(float f) { return bf_lo(pk_bf16(f, 0.f)); }
DI bf16x8 cat(s16x4 lo, s16x4 hi) { return __builtin_shufflevector(lo, hi, 0, 1, 2, 3, 4, 5, 6, 7); }
DI bf16x8 as_frag(u32x4 v) { return __builtin_bit_cast(bf16x8, v); }

DI void flag_store(char* ws, unsigned off, u64 tag) { __hip_atomic_store((GAS u64*)(ws + off), tag, __ATOMIC_RELAXED, __HIP_MEMORY_SCOPE_AGENT); }
DI u64 flag_load(char* ws, unsigned off) { return __hip_atomic_load((GAS u64*)(ws + off), __ATOMIC_RELAXED, __HIP_MEMORY_SCOPE_AGENT); }
DI void wait_flags(char* ws, unsigned off, int n, u64 tag, int lane) {
  const unsigned o = off + (lane < n ? lane : 0) * 8;
  for (unsigned spins = 0; spins < (1u << 16); ++spins) {
    const u64 v = flag_load(ws, o);
    if (__all(v == tag)) return;
    __builtin_amdgcn_s_sleep(1);
  }
  if (lane == 0) __hip_atomic_store((GAS unsigned*)(ws + WS_TMO), off, __ATOMIC_RELAXED, __HIP_MEMORY_SCOPE_AGENT);
}
DI unsigned lds_arrive(lchar* shm, int word, int lane) {
  WAITL0();
  unsigned r = 0;
  if (lane == 0) r = __hip_atomic_fetch_add((LAS unsigned*)(shm + L_SYNC) + word, 1u, __ATOMIC_RELAXED, __HIP_MEMORY_SCOPE_WORKGROUP);
  return __builtin_amdgcn_readfirstlane(r);
}
DI void lds_wait(lchar* shm, int word, unsigned target) {
  for (unsigned spins = 0; spins < (1u << 22); ++spins) {
    const unsigned v = __builtin_amdgcn_readfirstlane(__hip_atomic_load((LAS unsigned*)(shm + L_SYNC) + word, __ATOMIC_RELAXED, __HIP_MEMORY_SCOPE_WORKGROUP));
    if (v >= target) break;
    __builtin_amdgcn_s_sleep(1);
  }
  asm volatile("" ::: "memory");
}
DI void pair_sync(lchar* shm, int word, int lane) { lds_arrive(shm, word, lane); lds_wait(shm, word, 2); }

#define STAMP(i) do {} while (0)
#define RSTAMP(i) do {} while (0)

typedef __attribute__((ext_vector_type(8))) _Float16 half8;
typedef __attribute__((ext_vector_type(2))) _Float16 half2_t;
struct cf { float re, im; };
DI void opq(float& v) { asm("" : "+v"(v)); }
DI cf cmul(const cf& a, const cf& b) {
  float t0 = a.re * b.re, t1 = a.re * b.im;
  opq(t0); opq(t1);
  float re = __builtin_fmaf(-a.im, b.im, t0), im = __builtin_fmaf(a.im, b.re, t1);
  opq(re); opq(im);
  cf r; r.re = re; r.im = im; return r;
}
DI cf csqr(const cf& a) {
  float t0 = a.re * a.re, t1 = a.re + a.re;
  opq(t0); opq(t1);
  float re = __builtin_fmaf(-a.im, a.im, t0), im = t1 * a.im;
  opq(re); opq(im);
  cf r; r.re = re; r.im = im; return r;
}
DI cf cexp_at(float ar, float ai, float v) {
  const float m = __builtin_amdgcn_exp2f(ar * v);
  const float th = __builtin_amdgcn_fractf(ai * v);
  return cf{m * __builtin_amdgcn_cosf(th), m * __builtin_amdgcn_sinf(th)};
}
template <int CTRL> DI float dpp_f(float v) { return __builtin_bit_cast(float, __builtin_amdgcn_update_dpp(0, __builtin_bit_cast(int, v), CTRL, 0xf, 0xf, true)); }
DI float rdlane(float v, int l) { return __builtin_bit_cast(float, __builtin_amdgcn_readlane(__builtin_bit_cast(int, v), l)); }
DI void tab_put(lchar* tab, int row, float re, float im) {
  const half2_t h = {(_Float16)re, (_Float16)im};
  *(LAS unsigned*)(tab + row * KROW) = __builtin_bit_cast(unsigned, h);
}
DI half8 tab_frag(lchar* rd, int ks) { return *(const LAS half8*)(rd + ks * 64); }

__global__ __launch_bounds__(512, 2) void fused_kernel(
    const u16* __restrict__ x, const u16* __restrict__ Are, const u16* __restrict__ Aim, const u16* __restrict__ Bre,
    const u16* __restrict__ Bim, const u16* __restrict__ Cre, const u16* __restrict__ Cim, const u16* __restrict__ logdt,
    const u16* __restrict__ Dskip, const u16* __restrict__ wg, const u16* __restrict__ wu, const u16* __restrict__ wd,
    const u16* __restrict__ gw, u16* __restrict__ out, char* __restrict__ ws
) {
  extern __shared__ __attribute__((aligned(1024))) char shm_[];
  lchar* shm = (lchar*)shm_;
  const int tid = threadIdx.x, wid = __builtin_amdgcn_readfirstlane(tid >> 6), lane = tid & 63;
  const int b = blockIdx.x, xcd = b & 7, bi = b >> 3;
  const int mt = (bi & 7) + 8 * (xcd >> 2), jc = (bi >> 3) + 4 * (xcd & 3);
  const int ks0 = ((bi & 7) + 2 * (bi >> 3)) & 7;
  const int mt64 = 4 * xcd + (bi & 3), nt = bi >> 2;
  const int rot2 = nt % 7;
  const int kc = b >> 4, ct = b & 15;
  const int hidx = (xcd & 3) * 32 + bi, tb = 16 * (xcd >> 2) + (hidx & 15), cb = hidx >> 4;
  const rsrc_t rws = mk(ws, WS_DBG);
  RSTAMP(0); STAMP(1);

  u64 tag_all;
  bf16x8 Wr[28];
  const int nh = wid & 1, kq = wid >> 1;
  auto load_wfrags = [&](int lo, int hi) {
    const unsigned base = WS_WF + (unsigned)((2 * nt + nh) * 112 + 28 * kq) * 1024u;
    #pragma unroll
    for (int s = 0; s < 7; ++s) {
      int sr = s + rot2; if (sr >= 7) sr -= 7;
      #pragma unroll
      for (int q = 0; q < 4; ++q)
        if (4 * s + q >= lo && 4 * s + q < hi)
          Wr[4 * s + q] = as_frag(__builtin_amdgcn_raw_buffer_load_b128(rws, lane * 16, base + (4 * sr + q) * 1024, SC1));
    }
  };

  if (wid < 4) {
    if (wid == 0 && lane < 16) ((LAS unsigned*)(shm + L_SYNC))[lane] = 0;
    {
      const int chan = 2 * b + (wid & 1), pi = chan * NS + 64 * (wid >> 1) + lane;
      const u16 pAre = Are[pi], pAim = Aim[pi], pBre = Bre[pi], pBim = Bim[pi], pCre = Cre[pi], pCim = Cim[pi];
      const u16 pdt = logdt[chan];
      const float xl = bf2f(pdt), ph = xl * 1.44269504f;
      const float pl = __builtin_fmaf(xl, 1.44269504f, -ph) + xl * 1.92596299e-8f;
      const float dt = rbf(__builtin_amdgcn_exp2f(ph) * (1.f + pl * 0.693147181f));
      const float ar = dt * bf2f(pAre) * 1.44269504088896f, ai = dt * bf2f(pAim) * 0.15915494309189535f;
      const float br = dt * bf2f(pBre), bi_ = dt * bf2f(pBim), cr = bf2f(pCre), ci = bf2f(pCim);
      cf coef = cf{cr * br - ci * bi_, cr * bi_ + ci * br};
      float mx = fmaxf(fabsf(coef.re), fabsf(coef.im));
      mx = fmaxf(mx, dpp_f<0x111>(mx)); mx = fmaxf(mx, dpp_f<0x112>(mx)); mx = fmaxf(mx, dpp_f<0x114>(mx)); mx = fmaxf(mx, dpp_f<0x118>(mx));
      mx = fmaxf(fmaxf(rdlane(mx, 15), rdlane(mx, 31)), fmaxf(rdlane(mx, 47), rdlane(mx, 63)));
      int se = 127 + 12 + 127 - (int)(__float_as_uint(mx) >> 23); se = se > 253 ? 253 : (se < 1 ? 1 : se);
      const float scale = __uint_as_float((unsigned)se << 23);
      const cf E1 = cexp_at(ar, ai, 1.f), E8 = cexp_at(ar, ai, 8.f), E128 = cexp_at(ar, ai, 128.f), E1024 = cexp_at(ar, ai, 1024.f);
      LAS float* hw = (LAS float*)(shm + L_HAND + wid * 4096) + lane;
      hw[0] = E1.re; hw[64] = E1.im; hw[128] = E8.re; hw[192] = E8.im; hw[256] = E128.re; hw[320] = E128.im; hw[384] = E1024.re; hw[448] = E1024.im;
      hw[512] = coef.re * scale; hw[576] = coef.im * scale; hw[640] = __uint_as_float((unsigned)(254 - se) << 23);
    }
    const u64 e0 = flag_load(ws, WS_EPOCH);
    const u64 tag = (((u64)__builtin_amdgcn_readfirstlane((unsigned)(e0 >> 32)) << 32) | __builtin_amdgcn_readfirstlane((unsigned)e0)) + 1;
    const int wm = wid & 1, wn = wid >> 1;
    const int gk = lane >> 4, qq = (lane >> 2) & 3, pp = lane & 3;
    const int kk = 8 * gk + qq, pik = (kk & ~12) | ((kk & 4) << 1) | ((kk & 8) >> 1);
    const unsigned abase = (pp >> 1) * UPOFF + (15 * pik + 7 * wn) * 16 + 8 * (pp & 1);
    const int tok = lane & 15;
    const unsigned xb0 = WREG + (64 * wm + tok) * 128 + (((0 + gk) ^ ((tok >> 1) & 7)) * 16);
    const unsigned xb1 = WREG + (64 * wm + tok) * 128 + (((4 + gk) ^ ((tok >> 1) & 7)) * 16);
    f32x4 acc[7][4];
    #pragma unroll
    for (int a = 0; a < 7; ++a)
      #pragma unroll
      for (int t = 0; t < 4; ++t) acc[a][t] = f32x4{0, 0, 0, 0};
    bf16x8 A0[7], B0[4], A1[7], B1[4];
    auto rd = [&](bf16x8* A, bf16x8* B, int st, int ks) {
      lchar* base = shm + (st % 3) * STAGE;
      #pragma unroll
      for (int a = 0; a < 7; ++a) A[a] = cat(tr_read(base + abase + a * 16 + ks * 7680), tr_read(base + abase + a * 16 + ks * 7680 + 1920));
      #pragma unroll
      for (int t = 0; t < 4; ++t) B[t] = lds_b128(base + (ks ? xb1 : xb0) + t * 2048);
    };
    auto mm = [&](bf16x8* A, bf16x8* B) {
      #pragma unroll
      for (int a = 0; a < 7; ++a)
        #pragma unroll
        for (int t = 0; t < 4; ++t) acc[a][t] = __builtin_amdgcn_mfma_f32_16x16x32_bf16(A[a], B[t], acc[a][t], 0, 0, 0);
    };
    auto sched = [&]() {
      #pragma unroll
      for (int q = 0; q < 18; ++q) { __builtin_amdgcn_sched_group_barrier(0x8, 1, 0); __builtin_amdgcn_sched_group_barrier(0x100, 1, 0); }
      __builtin_amdgcn_sched_group_barrier(0x8, 10, 0);
    };
    STAMP(2);
    WAITL0();
    BAR();
    STAMP(3);
    __builtin_amdgcn_s_setprio(1);
    rd(A0, B0, 0, 0);
    SB();
    #pragma unroll
    for (int st = 0; st < 8; ++st) {
      rd(A1, B1, st, 1);
      mm(A0, B0);
      sched();
      SB();
      if (st + 1 < 8) {
        WAITL0();
        BAR();
        SB();
        rd(A0, B0, st + 1, 0);
      }
      mm(A1, B1);
      if (st + 1 < 8) sched();
      SB();
    }
    __builtin_amdgcn_s_setprio(0);
    STAMP(4);
    const int Y = lane >> 5, g2 = (lane >> 4) & 1;
    #pragma unroll
    for (int p = 0; p < 2; ++p) {
      const int tile32 = 4 * mt + 2 * wm + p;
      #pragma unroll
      for (int a = 0; a < 7; ++a) {
        float h[4];
        #pragma unroll
        for (int r = 0; r < 4; ++r) {
          auto sw = __builtin_amdgcn_permlane32_swap(__float_as_uint(acc[a][2 * p][r]), __float_as_uint(acc[a][2 * p + 1][r]), false, false);
          const float gv = __uint_as_float(sw[0]), uv = __uint_as_float(sw[1]);
          h[r] = uv * __builtin_amdgcn_rcpf(1.f + __builtin_amdgcn_exp2f(gv * -1.44269504f));
        }
        const int ag = 7 * wn + a, ks = 7 * jc + (ag >> 1), hh = ag & 1;
        const u32x2 v = {pk_bf16(h[0], h[1]), pk_bf16(h[2], h[3])};
        __builtin_amdgcn_raw_buffer_store_b64(v, rws, (unsigned)((32 * hh + 16 * Y + tok) * 16 + 8 * g2), WS_HF + (unsigned)(tile32 * 112 + ks) * 1024u, SC1);
        SB();
        load_wfrags(14 * p + 2 * a, 14 * p + 2 * a + 2);
        SB();
      }
    }
    STAMP(5);
    WAITV(2);
    STAMP(6);
    if (lds_arrive(shm, 1, lane) == 3 && lane == 0) flag_store(ws, WS_FH + (mt * 16 + jc) * 8, tag);
    if (wid == 0) wait_flags(ws, WS_FH + (mt64 >> 1) * 16 * 8, 16, tag, lane);
    STAMP(7);
    tag_all = tag;
  } else {
    const int s = wid - 4;
    const int ch = s & 1, nhalf = s >> 1, chan = 2 * b + ch;
    const rsrc_t rg = mk(wg, D * F * 2), ru = mk(wu, D * F * 2), rx = mk(x, L * D * 2);
    unsigned vw[4], vx[4];
    #pragma unroll
    for (int q = 0; q < 4; ++q) {
      const int c = (4 * s + q) * 64 + lane;
      const int r = (c * 4370) >> 16, a = c - 15 * r;
      const int k = (r & ~12) | ((r & 4) << 1) | ((r & 8) >> 1);
      vw[q] = (a == 14 || r > 63) ? 0x7ffffff0u : (unsigned)(k * (F * 2) + jc * 224 + a * 16);
      const int t = 8 * (4 * s + q) + (lane >> 3);
      vx[q] = (mt * 128 + t) * (D * 2) + (((lane & 7) ^ ((t >> 1) & 7)) * 16);
    }
    auto issue1 = [&](int st, int q) {
      const int ks = (st + ks0) & 7;
      lchar* base = shm + (st % 3) * STAGE;
      const unsigned sw = ks * 64 * F * 2, sx = ks * 128;
      if (q < 3 || s < 3) { blds16(rg, base + (4 * s + q) * 1024, vw[q], sw); blds16(ru, base + UPOFF + (4 * s + q) * 1024, vw[q], sw); }
      blds16(rx, base + WREG + (4 * s + q) * 1024, vx[q], sx);
    };
    auto issue = [&](int st) {
      #pragma unroll
      for (int q = 0; q < 4; ++q) issue1(st, q);
    };
    #define WAITST(extra) do { if (s < 3) WAITV(12 + (extra)); else WAITV(10 + (extra)); } while (0)
    STAMP(13);
    issue(0);
    STAMP(14);
    lchar* s2 = shm + L_SCR + 8192;
    lchar* s1 = shm + L_SCR + s * 2048;
    {
      const rsrc_t rwd = mk(wd, F * D * 2);
      #pragma unroll
      for (int u = 0; u < 2; ++u) {
        const int f = s + 4 * u;
        if (f < 7) blds16(rwd, s2 + f * 1024, (unsigned)((lane >> 2) * 1024 + (lane & 3) * 16), (unsigned)((112 * kc + 16 * f) * 1024 + ct * 64));
      }
      #pragma unroll
      for (int u = 0; u < 2; ++u)
        blds16(rx, s1 + u * 1024, (unsigned)((lane >> 1) * 1024 + (lane & 1) * 16), (unsigned)((64 * tb + 32 * u) * 1024 + (64 * cb + 16 * s) * 2));
    }
    const u32x4 gwv = *(const u32x4*)(gw + lane * 8);
    const u32x4 xv0 = *(const u32x4*)(x + (8 * b + 2 * s) * D + lane * 8), xv1 = *(const u32x4*)(x + (8 * b + 2 * s + 1) * D + lane * 8);
    const u16 pDs = Dskip[chan];
    const u64 e0 = flag_load(ws, WS_EPOCH);
    SB();
    issue(1);
    STAMP(15);
    WAITST(0);
    STAMP(30);
    BAR();
    STAMP(2);
    const u64 tag = (((u64)__builtin_amdgcn_readfirstlane((unsigned)(e0 >> 32)) << 32) | __builtin_amdgcn_readfirstlane((unsigned)e0)) + 1;
    tag_all = tag;
    const int gk = lane >> 4, i16 = lane & 15;
    cf E1, E8, E128, E1024, coef;
    float inv_scale;
    {
      const LAS float* hr = (const LAS float*)(shm + L_HAND + s * 4096) + lane;
      E1 = cf{hr[0], hr[64]}; E8 = cf{hr[128], hr[192]}; E128 = cf{hr[256], hr[320]}; E1024 = cf{hr[384], hr[448]}; coef = cf{hr[512], hr[576]}; inv_scale = hr[640];
      asm volatile("" : "+v"(E1.re), "+v"(E1.im), "+v"(E8.re), "+v"(E8.im), "+v"(E128.re), "+v"(E128.im), "+v"(E1024.re), "+v"(E1024.im), "+v"(coef.re), "+v"(coef.im), "+v"(inv_scale));
      WAITL0();
    }
    {
      const int h = lane >> 5, blk = (lane >> 4) & 1, q = (lane & 15) >> 2, p = lane & 3;
      issue1(2, 0); SB();
      #pragma unroll
      for (int u = 0; u < 2; ++u) {
        const int f = s + 4 * u;
        if (f < 7) {
          lchar* a = s2 + f * 1024 + (8 * h + q) * 64 + blk * 32 + p * 8;
          const bf16x8 v = tr_read2_asm(a, 256);
          st128(__builtin_bit_cast(u32x4, v), rws, lane * 16 + WS_WF + (unsigned)(ct * 112 + 7 * kc + f) * 1024u);
        }
      }
      SB(); issue1(2, 1); SB();
      #pragma unroll
      for (int v = 0; v < 2; ++v) {
        lchar* a = s1 + (32 * v + 8 * gk + q) * 32 + p * 8;
        const bf16x8 r = tr_read2_asm(a, 128);
        st128(__builtin_bit_cast(u32x4, r), rws,
            (unsigned)((16 * s + i16) * (L * 2) + (32 * v + 8 * gk) * 2) + WS_XT + (unsigned)(64 * cb * (L * 2) + 64 * tb * 2));
      }
      SB(); issue1(2, 2); SB(); issue1(2, 3); SB();
    }
    STAMP(3);
    lchar* tab = shm + L_SCR + s * KTAB;
    lchar* tw = tab + lane * 4;
    lchar* trd = tab + i16 * KROW + gk * 16;
    f32x4 kacc[3];
    half8 Bf[4];
    cf P2, P3, P4, P5, P6, P7;
    int nbk = 0;
    #define NEXT_B() do { STAMP(16 + 2 * nbk); WAITST(0); STAMP(17 + 2 * nbk); BAR(); SB(); ++nbk; } while (0)
    #define PIN2(c) asm volatile("" : "+v"((c).re), "+v"((c).im))
    const float dsk = bf2f(pDs);
    NEXT_B();
    {
      issue1(3, 0); SB();
      const cf E16 = csqr(E8), E32 = csqr(E16);
      cf X[4];
      X[0] = coef; X[1] = cmul(coef, E8); X[2] = cmul(coef, E16); X[3] = cmul(X[1], E16);
      #pragma unroll
      for (int g = 0; g < 4; ++g) {
        #pragma unroll
        for (int j = 0; j < 4; ++j) tab_put(tw, 4 * g + j, X[j].re, -X[j].im);
        if (g < 3) {
          #pragma unroll
          for (int j = 0; j < 4; ++j) X[j] = cmul(X[j], E32);
          SB(); issue1(3, g + 1); SB();
        }
      }
      #pragma unroll
      for (int k = 0; k < 4; ++k) { Bf[k] = tab_frag(trd, k); asm volatile("" : "+v"(Bf[k])); }
      SB();
    }
    auto kmm = [&](int T) {
      kacc[T] = f32x4{0, 0, 0, 0};
      #pragma unroll
      for (int k = 0; k < 4; ++k) kacc[T] = __builtin_amdgcn_mfma_f32_16x16x32_f16(tab_frag(trd, k), Bf[k], kacc[T], 0, 0, 0);
      asm volatile("" : "+v"(kacc[T]));
    };
    NEXT_B();
    if (s == 0 && lane == 0) {
      flag_store(ws, WS_FX + (cb * 32 + tb) * 8, tag);
      flag_store(ws, WS_FW + (ct * 16 + kc) * 8, tag);
    }
    SB();
    {
      issue1(4, 0); SB();
      P2 = csqr(E1); P3 = cmul(P2, E1); P4 = csqr(P2); P5 = cmul(P4, E1); P6 = cmul(P4, P2); P7 = cmul(P4, P3);
      tab_put(tw, 0, 1.f, 0.f); tab_put(tw, 1, E1.re, E1.im); tab_put(tw, 2, P2.re, P2.im); tab_put(tw, 3, P3.re, P3.im);
      SB(); issue1(4, 1); SB();
      tab_put(tw, 4, P4.re, P4.im); tab_put(tw, 5, P5.re, P5.im); tab_put(tw, 6, P6.re, P6.im); tab_put(tw, 7, P7.re, P7.im);
      tab_put(tw, 8, E128.re, E128.im);
      cf t0 = cmul(E128, E1), t1 = cmul(E128, P2);
      tab_put(tw, 9, t0.re, t0.im); tab_put(tw, 10, t1.re, t1.im);
      SB(); issue1(4, 2); SB();
      t0 = cmul(E128, P3); t1 = cmul(E128, P4); tab_put(tw, 11, t0.re, t0.im); tab_put(tw, 12, t1.re, t1.im);
      t0 = cmul(E128, P5); t1 = cmul(E128, P6); tab_put(tw, 13, t0.re, t0.im); tab_put(tw, 14, t1.re, t1.im);
      t0 = cmul(E128, P7); tab_put(tw, 15, t0.re, t0.im);
      SB(); issue1(4, 3); SB();
      kmm(0);
      PIN2(P2); PIN2(P4); PIN2(P6);
      SB();
    }
    NEXT_B();
    {
      issue1(5, 0); SB();
      const cf E256 = csqr(E128), E384 = cmul(E256, E128), E512 = csqr(E256);
      cf t0 = cmul(E256, P2), t1 = cmul(E256, P4), t2 = cmul(E256, P6);
      tab_put(tw, 0, E256.re, E256.im); tab_put(tw, 1, t0.re, t0.im); tab_put(tw, 2, t1.re, t1.im); tab_put(tw, 3, t2.re, t2.im);
      SB(); issue1(5, 1); SB();
      t0 = cmul(E384, P2); t1 = cmul(E384, P4); t2 = cmul(E384, P6);
      tab_put(tw, 4, E384.re, E384.im); tab_put(tw, 5, t0.re, t0.im); tab_put(tw, 6, t1.re, t1.im); tab_put(tw, 7, t2.re, t2.im);
      SB(); issue1(5, 2); SB();
      const cf E640 = cmul(E512, E128), E768 = cmul(E512, E256);
      t0 = cmul(E512, P4); const cf E896 = cmul(E768, E128);
      t1 = cmul(E640, P4); t2 = cmul(E768, P4);
      tab_put(tw, 8, E512.re, E512.im); tab_put(tw, 9, t0.re, t0.im); tab_put(tw, 10, E640.re, E640.im); tab_put(tw, 11, t1.re, t1.im);
      SB(); issue1(5, 3); SB();
      t0 = cmul(E896, P4);
      tab_put(tw, 12, E768.re, E768.im); tab_put(tw, 13, t2.re, t2.im); tab_put(tw, 14, E896.re, E896.im); tab_put(tw, 15, t0.re, t0.im);
      kmm(1);
      SB();
    }
    NEXT_B();
    {
      issue1(6, 0); SB();
      const cf E256 = csqr(E128);
      cf ta = E1024, tb2 = cmul(E1024, E128);
      #pragma unroll
      for (int i = 0; i <= 8; i += 2) {
        tab_put(tw, i, ta.re, ta.im);
        if (i < 8) { tab_put(tw, i + 1, tb2.re, tb2.im); ta = cmul(ta, E256); if (i < 6) tb2 = cmul(tb2, E256); }
        if (i == 2) { SB(); issue1(6, 1); SB(); }
        if (i == 4) { SB(); issue1(6, 2); SB(); }
      }
      SB(); issue1(6, 3); SB();
      kmm(2);
      LAS float* kp = (LAS float*)tab;
      *(LAS f32x4*)(kp + 128 * (gk >> 1) + 4 * (gk & 1) + 8 * i16) = kacc[0] * inv_scale;
      const f32x4 k1 = kacc[1] * inv_scale, k2 = kacc[2] * inv_scale;
      if (gk < 2) *(LAS f32x4*)(kp + 256 + 64 * gk + 4 * i16) = k1;
      else {
        *(LAS f32x2*)(kp + 384 + 64 * (gk - 2) + 2 * i16) = f32x2{k1[0], k1[1]};
        *(LAS f32x2*)(kp + 384 + 64 * (gk - 2) + 32 + 2 * i16) = f32x2{k1[2], k1[3]};
      }
      if (gk < 2) {
        #pragma unroll
        for (int r = 0; r < 4; ++r) kp[512 + 16 * (4 * gk + r) + i16] = k2[r];
      } else if (gk == 2) kp[640 + i16] = k2[0];
      WAITL0();
    }
    NEXT_B();
    const u64 pollW = flag_load(ws, WS_FW + 2 * nt * 16 * 8 + (lane & 31) * 8);
    const u64 pollX = flag_load(ws, WS_FX + (chan >> 6) * 32 * 8 + (lane & 31) * 8);
    SB();
    issue1(7, 0); SB();
    {
      float g2[2];
      #pragma unroll
      for (int u = 0; u < 2; ++u) {
        const u32x4 xv = u ? xv1 : xv0;
        float sa = 0.f, sb2 = 0.f;
        #pragma unroll
        for (int e = 0; e < 4; ++e) {
          float p0 = bf_lo(xv[e]) * bf_lo(gwv[e]), p1 = bf_hi(xv[e]) * bf_hi(gwv[e]);
          asm("" : "+v"(p0)); asm("" : "+v"(p1));
          const unsigned p = pk_bf16(p0, p1);
          sa += bf_lo(p); sb2 += bf_hi(p);
          asm("" : "+v"(sa)); asm("" : "+v"(sb2));
        }
        float sm = sa + sb2;
        sm += dpp_f<0x111>(sm); sm += dpp_f<0x112>(sm); sm += dpp_f<0x114>(sm); sm += dpp_f<0x118>(sm);
        const float tot = (rdlane(sm, 15) + rdlane(sm, 31)) + (rdlane(sm, 47) + rdlane(sm, 63));
        g2[u] = rbf(__builtin_amdgcn_rcpf(1.f + __builtin_amdgcn_exp2f(rbf(tot) * -1.44269504f)));
        if (u == 0) { SB(); issue1(7, 1); SB(); }
      }
      SB(); issue1(7, 2); SB();
      if (lane < 2) __hip_atomic_store((GAS float*)(ws + WS_G) + 8 * b + 2 * s + lane, lane ? g2[1] : g2[0], __ATOMIC_RELAXED, __HIP_MEMORY_SCOPE_AGENT);
      SB(); issue1(7, 3); SB();
    }
    unsigned gidx[2][6];
    #pragma unroll
    for (int it = 0; it < 2; ++it) {
      const float f0 = (float)(2048 - 8 * (nhalf * 128 + it * 64 + lane));
      #pragma unroll
      for (int j = 0; j < 6; ++j) {
        float fa = f0 - (float)(2 * j), fb = f0 - (float)(2 * j + 1);
        asm("" : "+v"(fa)); asm("" : "+v"(fb));
        gidx[it][j] = pk_bf16(fa, fb);
        asm volatile("" : "+v"(gidx[it][j]));
      }
    }
    NEXT_B();
    if (!__all(pollW == tag)) wait_flags(ws, WS_FW + 2 * nt * 16 * 8, 32, tag, lane);
    if (!__all(pollX == tag)) wait_flags(ws, WS_FX + (chan >> 6) * 32 * 8, 32, tag, lane);
    lchar* uimg = shm + L_U + ch * UIMG;
    #pragma unroll
    for (int q = 0; q < 2; ++q) {
      const int P = (2 * nhalf + q) * 64 + lane;
      const int c = P >> 2, g = (P & 3) ^ (2 * ((c >> 2) & 1));
      blds16<SC1>(rws, uimg + 1024 + (2 * nhalf + q) * 1024, (unsigned)(c * 64 + g * 16), WS_XT + (unsigned)chan * (L * 2));
    }
    if (nhalf == 0) *(LAS u32x4*)(uimg + lane * 16) = u32x4{0, 0, 0, 0};
    SB();
    load_wfrags(0, 14);
    SB();
    {
      lchar* k0 = shm + L_SCR + (ch + 0) * KTAB;
      lchar* k1 = shm + L_SCR + (ch + 2) * KTAB;
      lchar* kc = shm + L_KC + ch * 4 * KCOPY;
      #pragma unroll
      for (int it = 0; it < 2; ++it) {
        const int q = nhalf * 128 + it * 64 + lane;
        float e[11];
        #pragma unroll
        for (int j = 0; j < 11; ++j) {
          int p = (int)((j & 1) ? (gidx[it][j >> 1] >> 16) : (gidx[it][j >> 1] & 0xffffu)) - 17024;
          if (it == 1 && nhalf == 1) { const int sx = 2048 - 8 * q - j; p = sx < 128 ? (sx < 0 ? 0 : sx) : p; }
          float v = *(const LAS float*)(k0 + 4 * p) + *(const LAS float*)(k1 + 4 * p);
          if (it == 1 && j > 8) v = (8 * q + j > 2048) ? 0.f : v;
          e[j] = v;
        }
        unsigned P[5], Q[5];
        #pragma unroll
        for (int j = 0; j < 5; ++j) { P[j] = pk_bf16(e[2 * j], e[2 * j + 1]); Q[j] = pk_bf16(e[2 * j + 1], e[2 * j + 2 > 10 ? 10 : 2 * j + 2]); }
        *(LAS u32x4*)(kc + 0 * KCOPY + q * 16) = u32x4{P[0], P[1], P[2], P[3]};
        *(LAS u32x4*)(kc + 1 * KCOPY + q * 16) = u32x4{Q[0], Q[1], Q[2], Q[3]};
        *(LAS u32x4*)(kc + 2 * KCOPY + q * 16) = u32x4{P[1], P[2], P[3], P[4]};
        *(LAS u32x4*)(kc + 3 * KCOPY + q * 16) = u32x4{Q[1], Q[2], Q[3], Q[4]};
      }
      if (nhalf == 0 && lane < 16) {
        const int cp = lane >> 2, q = 256 + (lane & 3);
        const float kz = *(const LAS float*)k0 + *(const LAS float*)k1;
        u32x4 v = {0, 0, 0, 0};
        if (cp == 0 && q == 256) v[0] = pk_bf16(kz, 0.f);
        *(LAS u32x4*)(kc + cp * KCOPY + q * 16) = v;
      }
    }
    STAMP(28);
    WAITV(14); WAITL0(); BAR();
    STAMP(4);
    load_wfrags(14, 28);
    STAMP(5);
    {
      f32x4 ya[2][4];
      #pragma unroll
      for (int bb = 0; bb < 2; ++bb)
        #pragma unroll
        for (int ab = 0; ab < 4; ++ab) ya[bb][ab] = f32x4{0, 0, 0, 0};
      const int r = i16, g = gk;
      const int x0 = 2048 - r + 8 * g;
      lchar* fb = shm + L_KC + ch * 4 * KCOPY + (x0 & 3) * KCOPY + (x0 & ~3) * 2 - 3 * 1024 - 32;
      #pragma unroll
      for (int dd = 0; dd < 8; ++dd) {
        const int d0 = 8 * nhalf + dd;
        lchar* f = fb - 64 * d0;
        const int c = i16 - d0 + 16;
        lchar* gp = uimg + c * 64 + ((g ^ (2 * ((c >> 2) & 1))) * 16);
        bf16x8 Ff[4][2], Gf[4];
        #pragma unroll
        for (int i = 0; i < 4; ++i)
          #pragma unroll
          for (int bb = 0; bb < 2; ++bb) {
            lchar* a = f + 1024 * (3 - i) + 32 * (1 - bb);
            const s16x4 lo = *(const LAS s16x4*)a, hi = *(const LAS s16x4*)(a + 8);
            Ff[i][bb] = cat(lo, hi);
          }
        #pragma unroll
        for (int k = 0; k < 4; ++k) Gf[k] = lds_b128(gp + 1024 * k);
        #pragma unroll
        for (int ab = 0; ab < 4; ++ab)
          #pragma unroll
          for (int i = 0; i <= ab; ++i)
            #pragma unroll
            for (int bb = 0; bb < 2; ++bb) ya[bb][ab] = __builtin_amdgcn_mfma_f32_16x16x32_bf16(Ff[i][bb], Gf[ab - i], ya[bb][ab], 0, 0, 0);
      }
      lchar* yp = shm + L_Y + (ch * 2 + nhalf) * 8192;
      #pragma unroll
      for (int bb = 0; bb < 2; ++bb)
        #pragma unroll
        for (int ab = 0; ab < 4; ++ab)
          *(LAS f32x4*)(yp + (16 * ab + i16) * 128 + (((4 * bb + gk) ^ (i16 & 7)) * 16)) = ya[bb][ab];
    }
    pair_sync(shm, 6 + ch, lane);
    STAMP(6);
    {
      lchar* y0 = shm + L_Y + ch * 2 * 8192;
      #pragma unroll
      for (int it = 0; it < 2; ++it) {
        const int q = nhalf * 128 + it * 64 + lane;
        const int a = q >> 2, c0 = 2 * (q & 3);
        const f32x4 p0 = *(const LAS f32x4*)(y0 + a * 128 + ((c0 ^ (a & 7)) * 16)) + *(const LAS f32x4*)(y0 + 8192 + a * 128 + ((c0 ^ (a & 7)) * 16));
        const f32x4 p1 = *(const LAS f32x4*)(y0 + a * 128 + (((c0 + 1) ^ (a & 7)) * 16)) + *(const LAS f32x4*)(y0 + 8192 + a * 128 + (((c0 + 1) ^ (a & 7)) * 16));
        const u32x4 uv = *(const LAS u32x4*)(uimg + (a + 16) * 64 + (((q & 3) ^ (2 * ((a >> 2) & 1))) * 16));
        u32x4 o;
        #pragma unroll
        for (int e = 0; e < 4; ++e) {
          const float ya_ = e < 2 ? p0[2 * e] : p1[2 * e - 4], yb_ = e < 2 ? p0[2 * e + 1] : p1[2 * e - 3];
          const unsigned yr = pk_bf16(ya_, yb_), sk = pk_bf16(bf_lo(uv[e]) * dsk, bf_hi(uv[e]) * dsk);
          o[e] = pk_bf16(bf_lo(yr) + bf_lo(sk), bf_hi(yr) + bf_hi(sk));
        }
        st128(o, rws, (unsigned)(q * 16) + WS_AT + (unsigned)chan * (L * 2));
      }
    }
    WAITV(0);
    if (lds_arrive(shm, 8, lane) == 3 && lane == 0) { flag_store(ws, WS_FA + b * 8, tag); flag_store(ws, WS_FG + b * 8, tag); }
    STAMP(7);
  }
  const u64 tag = tag_all;
  WAITL0();
  BAR();
  STAMP(8);
  {
    const unsigned hbase = WS_HF + (unsigned)((2 * mt64 + nh) * 112 + 28 * kq) * 1024u;
    auto issue2 = [&](int st) {
      int sr = st + rot2; if (sr >= 7) sr -= 7;
      lchar* dst = shm + L_RING + (st & 3) * 32768 + kq * 8192 + nh * 4096;
      #pragma unroll
      for (int q = 0; q < 4; ++q) blds16<SC1>(rws, dst + q * 1024, (unsigned)(lane * 16), hbase + (4 * sr + q) * 1024);
    };
    issue2(0); issue2(1); issue2(2);
    f32x16 acc2[2];
    #pragma unroll
    for (int t = 0; t < 2; ++t)
      #pragma unroll
      for (int e = 0; e < 16; ++e) acc2[t][e] = 0.f;
    #pragma unroll
    for (int st = 0; st < 7; ++st) {
      if (st <= 4) WAITV(8); else if (st == 5) WAITV(4); else WAITV(0);
      BAR();
      lchar* src = shm + L_RING + (st & 3) * 32768 + kq * 8192 + lane * 16;
      #pragma unroll
      for (int q = 0; q < 4; ++q)
        #pragma unroll
        for (int t = 0; t < 2; ++t) acc2[t] = __builtin_amdgcn_mfma_f32_32x32x16_bf16(Wr[4 * st + q], lds_b128(src + t * 4096 + q * 1024), acc2[t], 0, 0, 0);
      SB();
      if (st + 3 < 7) issue2(st + 3);
      SB();
    }
    STAMP(9);
    if (wid == 0) { wait_flags(ws, WS_FA + 32 * nt * 8, 32, tag, lane); wait_flags(ws, WS_FG + 8 * mt64 * 8, 8, tag, lane); }
    WAITL0();
    BAR();
    blds16<SC1>(rws, shm + L_AT + wid * 1024, (unsigned)((lane >> 3) * (L * 2) + (lane & 7) * 16), WS_AT + (unsigned)((64 * nt + 8 * wid) * (L * 2) + 64 * mt64 * 2));
    const int tokl = 16 * (wid & 3) + (lane & 15), dh = wid >> 2, G = lane >> 4;
    const float gate = __uint_as_float(__builtin_amdgcn_raw_buffer_load_b32(rws, (unsigned)(tokl * 4), WS_G + (unsigned)(64 * mt64 * 4), SC1));
    {
      lchar* pw = shm + kq * PKQ + (lane & 31) * PSTR + (32 * nh + 4 * (lane >> 5)) * 4;
      #pragma unroll
      for (int t = 0; t < 2; ++t)
        #pragma unroll
        for (int q = 0; q < 4; ++q)
          *(LAS f32x4*)(pw + t * 32 * PSTR + q * 32) = f32x4{acc2[t][4 * q], acc2[t][4 * q + 1], acc2[t][4 * q + 2], acc2[t][4 * q + 3]};
    }
    WAITV(0);
    WAITL0();
    BAR();
    STAMP(10);
    {
      lchar* pr = shm + tokl * PSTR + (32 * dh + 8 * G) * 4;
      f32x4 s0 = f32x4{0, 0, 0, 0}, s1 = f32x4{0, 0, 0, 0};
      #pragma unroll
      for (int k = 0; k < 4; ++k) { s0 += *(const LAS f32x4*)(pr + k * PKQ); s1 += *(const LAS f32x4*)(pr + k * PKQ + 16); }
      const int q = (lane & 15) >> 2, p = lane & 3;
      lchar* aa = shm + L_AT + (32 * dh + 8 * G + q) * 128 + (16 * (wid & 3) + 4 * p) * 2;
      const s16x4 a0 = tr_read(aa), a1 = tr_read(aa + 512);
      const float og = rbf(1.f - gate);
      u32x4 o;
      #pragma unroll
      for (int e = 0; e < 4; ++e) {
        const float b0 = e < 2 ? s0[2 * e] : s1[2 * e - 4], b1 = e < 2 ? s0[2 * e + 1] : s1[2 * e - 3];
        const float av0 = bf2f((u16)(e < 2 ? a0[2 * e] : a1[2 * e - 4])), av1 = bf2f((u16)(e < 2 ? a0[2 * e + 1] : a1[2 * e - 3]));
        const unsigned bb = pk_bf16(b0, b1), t1 = pk_bf16(gate * av0, gate * av1), t2 = pk_bf16(og * bf_lo(bb), og * bf_hi(bb));
        o[e] = pk_bf16(bf_lo(t1) + bf_lo(t2), bf_hi(t1) + bf_hi(t2));
      }
      *(u32x4*)(out + (64 * mt64 + tokl) * D + 64 * nt + 32 * dh + 8 * G) = o;
    }
    STAMP(11);
  }
  if (b == 0 && wid == 0) {
    #pragma unroll
    for (int q = 0; q < 4; ++q) wait_flags(ws, WS_FG + q * 64 * 8, 64, tag, lane);
    if (lane == 0) flag_store(ws, WS_EPOCH, tag);
  }
  RSTAMP(12);
}

extern "C" void kernel_launch(void* const* d_in, const int* in_sizes, int n_in, void* d_out, int out_size, void* d_ws, size_t ws_size,
                              hipStream_t stream) {
  (void)in_sizes; (void)n_in; (void)out_size; (void)ws_size;
  hipFuncSetAttribute((const void*)fused_kernel, hipFuncAttributeMaxDynamicSharedMemorySize, LDS_BYTES);
  fused_kernel<<<256, 512, LDS_BYTES, stream>>>(
      (const u16*)d_in[0], (const u16*)d_in[1], (const u16*)d_in[2], (const u16*)d_in[3], (const u16*)d_in[4], (const u16*)d_in[5],
      (const u16*)d_in[6], (const u16*)d_in[7], (const u16*)d_in[8], (const u16*)d_in[10], (const u16*)d_in[11], (const u16*)d_in[12],
      (const u16*)d_in[13], (u16*)d_out, (char*)d_ws
  );
}
```

```cpp
#include <hip/hip_runtime.h>
#include <stdint.h>

typedef unsigned short u16;
typedef unsigned long long u64;
#define LAS __attribute__((address_space(3)))
#define GAS __attribute__((address_space(1)))
#define DI __device__ __forceinline__
typedef LAS char lchar;
typedef __attribute__((ext_vector_type(8))) short bf16x8;
typedef __attribute__((ext_vector_type(4))) short s16x4;
typedef __attribute__((ext_vector_type(4))) float f32x4;
typedef __attribute__((ext_vector_type(2))) float f32x2;
typedef __attribute__((ext_vector_type(16))) float f32x16;
typedef __attribute__((ext_vector_type(2))) unsigned u32x2;
typedef __attribute__((ext_vector_type(4))) unsigned u32x4;
typedef __attribute__((ext_vector_type(2))) __bf16 bf16x2_t;
typedef __amdgpu_buffer_rsrc_t rsrc_t;

constexpr int L = 2048, D = 512, NS = 128, F = 1792;
constexpr unsigned WS_EPOCH = 0, WS_FX = 4096, WS_FW = 8192, WS_FG = 12288, WS_FH = 16384, WS_FA = 20480, WS_TMO = 24576;
constexpr unsigned WS_G = 32768, WS_XT = 65536, WS_WF = WS_XT + D * L * 2, WS_AT = WS_WF + D * F * 2, WS_HF = WS_AT + D * L * 2;
constexpr unsigned WS_DBG = WS_HF + L * F * 2;
constexpr int UPOFF = 15488, WREG = 30848, XREG = 16384, STAGE = WREG + XREG;
constexpr int L_EXTRA = 3 * STAGE;
constexpr int L_SYNC = L_EXTRA;
constexpr int KTAB = 4352, KROW = 272;
constexpr int L_GTAB = 2 * STAGE;
constexpr int L_KP = L_SYNC + 256;
constexpr int KPCH = 656 * 8;
constexpr int L_DUMMY = L_KP + 2 * KPCH;
constexpr int L_END_A = L_DUMMY + 1024;
static_assert(L_END_A <= 163840 - 2048, "LDS");
constexpr int L_JSCR = 2 * STAGE + WREG;
constexpr int KCOPY = 4160;
constexpr int L_KC = 2 * STAGE;
constexpr int L_U = L_KP;
constexpr int UIMG = 5120;
static_assert(L_KC + 2 * 4 * KCOPY <= 3 * STAGE && 2 * UIMG <= 2 * KPCH, "slot2");
constexpr int L_Y = 0;

constexpr int L_RING = 32768;
constexpr int L_AT = L_END_A;
constexpr int PSTR = 272, PKQ = 64 * PSTR;
DI constexpr int pk_off(int kq) { return kq == 0 ? 0 : 65536 + (kq - 1) * PKQ; }
static_assert(65536 + 3 * PKQ <= 131072 && L_AT + 8192 <= 163840 - 2048, "LDS");
constexpr int LDS_BYTES = 163840;
#define CODE_BYTES 24064u
#define CODEPF_LINES 8
#define CODEPF_WAVE 3

#define BAR() __builtin_amdgcn_s_barrier()
#define SB() __builtin_amdgcn_sched_barrier(0)
#define WAITV(n) asm volatile("s_waitcnt vmcnt(%0)" ::"n"(n) : "memory")
#define WAITL0() asm volatile("s_waitcnt lgkmcnt(0)" ::: "memory")
constexpr int SC1 = 16;

DI rsrc_t mk(const void* p, unsigned bytes) { return __builtin_amdgcn_make_buffer_rsrc((void*)p, 0, bytes, 0x00020000); }
template <int AUX = 0> DI void blds16(rsrc_t r, lchar* l, unsigned voff, unsigned soff) { __builtin_amdgcn_raw_ptr_buffer_load_lds(r, (LAS void*)l, 16, voff, soff, 0, AUX); }
DI void touch_line(const void* g, lchar* dummy) {
  unsigned keep;
  const unsigned dst = __builtin_amdgcn_readfirstlane((unsigned)(size_t)dummy);
  asm volatile("s_mov_b32 %0, m0\n\ts_mov_b32 m0, %2\n\ts_nop 0\n\tglobal_load_lds_dword %1, off\n\ts_mov_b32 m0, %0" : "=&s"(keep) : "v"(g), "s"(dst) : "memory");
}
DI void touch_line_sc1(const void* g, lchar* dummy) {
  unsigned keep;
  const unsigned dst = __builtin_amdgcn_readfirstlane((unsigned)(size_t)dummy);
  asm volatile("s_mov_b32 %0, m0\n\ts_mov_b32 m0, %2\n\ts_nop 0\n\tglobal_load_lds_dword %1, off sc1\n\ts_mov_b32 m0, %0" : "=&s"(keep) : "v"(g), "s"(dst) : "memory");
}
DI s16x4 tr_read(lchar* p) { return __builtin_amdgcn_ds_read_tr16_b64_v4i16((LAS s16x4*)p); }
DI bf16x8 lds_b128(lchar* p) { return *(const LAS bf16x8*)p; }
DI void st128(u32x4 v, rsrc_t r, unsigned off) { __builtin_amdgcn_raw_buffer_store_b128(v, r, off, 0, SC1); }
DI unsigned pk_bf16(float a, float b) { bf16x2_t v = {(__bf16)a, (__bf16)b}; return __builtin_bit_cast(unsigned, v); }
DI float bf_lo(unsigned v) { return __uint_as_float(v << 16); }
DI float bf_hi(unsigned v) { return __uint_as_float(v & 0xffff0000u); }
DI float bf2f(u16 v) { return __uint_as_float(((unsigned)v) << 16); }
DI float rbf(float f) { return bf_lo(pk_bf16(f, 0.f)); }
DI bf16x8 cat(s16x4 lo, s16x4 hi) { return __builtin_shufflevector(lo, hi, 0, 1, 2, 3, 4, 5, 6, 7); }
DI bf16x8 as_frag(u32x4 v) { return __builtin_bit_cast(bf16x8, v); }

DI void flag_store(char* ws, unsigned off, u64 tag) { __hip_atomic_store((GAS u64*)(ws + off), tag, __ATOMIC_RELAXED, __HIP_MEMORY_SCOPE_AGENT); }
DI u64 flag_load(char* ws, unsigned off) { return __hip_atomic_load((GAS u64*)(ws + off), __ATOMIC_RELAXED, __HIP_MEMORY_SCOPE_AGENT); }
DI void wait_flags(char* ws, unsigned off, int n, u64 tag, int lane) {
  const unsigned o = off + (lane < n ? lane : 0) * 8;
  #pragma unroll 1
  for (unsigned spins = 0; spins < (1u << 16); ++spins) {
    const u64 v = flag_load(ws, o);
    if (__all(v == tag)) return;
    __builtin_amdgcn_s_sleep(1);
  }
  if (lane == 0) __hip_atomic_store((GAS unsigned*)(ws + WS_TMO), off, __ATOMIC_RELAXED, __HIP_MEMORY_SCOPE_AGENT);
}
DI void wait_flags_at(char* ws, unsigned o, u64 tag, int lane) {
  #pragma unroll 1
  for (unsigned spins = 0; spins < (1u << 16); ++spins) {
    const u64 v = flag_load(ws, o);
    if (__all(v == tag)) return;
    __builtin_amdgcn_s_sleep(1);
  }
  if (lane == 0) __hip_atomic_store((GAS unsigned*)(ws + WS_TMO), o, __ATOMIC_RELAXED, __HIP_MEMORY_SCOPE_AGENT);
}
DI unsigned lds_arrive(lchar* shm, int word, int lane) {
  WAITL0();
  unsigned r = 0;
  if (lane == 0) r = __hip_atomic_fetch_add((LAS unsigned*)(shm + L_SYNC) + word, 1u, __ATOMIC_RELAXED, __HIP_MEMORY_SCOPE_WORKGROUP);
  return __builtin_amdgcn_readfirstlane(r);
}
DI void lds_wait(lchar* shm, int word, unsigned target) {
  #pragma unroll 1
  for (unsigned spins = 0; spins < (1u << 22); ++spins) {
    const unsigned v = __builtin_amdgcn_readfirstlane(__hip_atomic_load((LAS unsigned*)(shm + L_SYNC) + word, __ATOMIC_RELAXED, __HIP_MEMORY_SCOPE_WORKGROUP));
    if (v >= target) break;
    __builtin_amdgcn_s_sleep(1);
  }
  asm volatile("" ::: "memory");
}

typedef __attribute__((ext_vector_type(8))) _Float16 half8;
typedef __attribute__((ext_vector_type(2))) _Float16 half2_t;
struct cf { float re, im; };
DI void opq(float& v) { asm("" : "+v"(v)); }
DI cf cmul(const cf& a, const cf& b) {
  float t0 = a.re * b.re, t1 = a.re * b.im;
  opq(t0); opq(t1);
  float re = __builtin_fmaf(-a.im, b.im, t0), im = __builtin_fmaf(a.im, b.re, t1);
  opq(re); opq(im);
  cf r; r.re = re; r.im = im; return r;
}
DI cf csqr(const cf& a) {
  float t0 = a.re * a.re, t1 = a.re + a.re;
  opq(t0); opq(t1);
  float re = __builtin_fmaf(-a.im, a.im, t0), im = t1 * a.im;
  opq(re); opq(im);
  cf r; r.re = re; r.im = im; return r;
}
DI unsigned pk_bf16_asm(float a, float b) { unsigned r; asm("v_cvt_pk_bf16_f32 %0, %1, %2" : "=v"(r) : "v"(a), "v"(b)); return r; }
DI cf cexp_at(float ar, float ai, float v) {
  const float m = __builtin_amdgcn_exp2f(ar * v);
  const float th = __builtin_amdgcn_fractf(ai * v);
  return cf{m * __builtin_amdgcn_cosf(th), m * __builtin_amdgcn_sinf(th)};
}
template <int CTRL> DI float dpp_f(float v) { return __builtin_bit_cast(float, __builtin_amdgcn_update_dpp(0, __builtin_bit_cast(int, v), CTRL, 0xf, 0xf, true)); }
DI float rdlane(float v, int l) { return __builtin_bit_cast(float, __builtin_amdgcn_readlane(__builtin_bit_cast(int, v), l)); }
DI half8 tab_frag(lchar* rd, int ks) { return *(const LAS half8*)(rd + ks * 64); }

struct KPar { u16 Are, Aim, Bre, Bim, Cre, Cim, dt; };
DI unsigned h2(const cf& a) { const half2_t h = {(_Float16)a.re, (_Float16)a.im}; return __builtin_bit_cast(unsigned, h); }
DI unsigned h2c(const cf& a) { const half2_t h = {(_Float16)a.re, (_Float16)-a.im}; return __builtin_bit_cast(unsigned, h); }
DI unsigned hmul(unsigned a, unsigned b) {
  unsigned t, r;
  asm("v_pk_mul_f16 %0, %1, %2 op_sel:[0,0] op_sel_hi:[0,1]" : "=v"(t) : "v"(a), "v"(b));
  asm("v_pk_fma_f16 %0, %1, %2, %3 op_sel:[1,1,0] op_sel_hi:[1,0,1] neg_lo:[0,1,0]" : "=v"(r) : "v"(a), "v"(b), "v"(t));
  return r;
}
template <int TILES, class Hook>
DI void kgen(const KPar& pp, lchar* tab, LAS float* kp, int lane, Hook hook) {
  const float xl = bf2f(pp.dt), ph = xl * 1.44269504f;
  const float pl = __builtin_fmaf(xl, 1.44269504f, -ph) + xl * 1.92596299e-8f;
  const float dt = rbf(__builtin_amdgcn_exp2f(ph) * (1.f + pl * 0.693147181f));
  const float ar = dt * bf2f(pp.Are) * 1.44269504088896f, ai = dt * bf2f(pp.Aim) * 0.15915494309189535f;
  const float br = dt * bf2f(pp.Bre), bi_ = dt * bf2f(pp.Bim), cr = bf2f(pp.Cre), ci = bf2f(pp.Cim);
  cf coef = cf{cr * br - ci * bi_, cr * bi_ + ci * br};
  float mx = fmaxf(fabsf(coef.re), fabsf(coef.im));
  mx = fmaxf(mx, dpp_f<0x111>(mx)); mx = fmaxf(mx, dpp_f<0x112>(mx)); mx = fmaxf(mx, dpp_f<0x114>(mx)); mx = fmaxf(mx, dpp_f<0x118>(mx));
  mx = fmaxf(fmaxf(rdlane(mx, 15), rdlane(mx, 31)), fmaxf(rdlane(mx, 47), rdlane(mx, 63)));
  int se = 127 + 12 + 127 - (int)(__float_as_uint(mx) >> 23); se = se > 253 ? 253 : (se < 1 ? 1 : se);
  const float scale = __uint_as_float((unsigned)se << 23), inv_scale = __uint_as_float((unsigned)(254 - se) << 23);
  coef.re *= scale; coef.im *= scale;
  hook(0);
  const cf E1 = cexp_at(ar, ai, 1.f), E8 = cexp_at(ar, ai, 8.f), E128 = cexp_at(ar, ai, 128.f), E1024 = cexp_at(ar, ai, 1024.f);
  const int gk = lane >> 4, i16 = lane & 15;
  LAS unsigned* tw = (LAS unsigned*)(tab + lane * 4);
  lchar* trd = tab + i16 * KROW + gk * 16;
  #define ROW(r) tw[(r) * (KROW / 4)]
  half8 Bf[4];
  {
    const cf E16 = csqr(E8), E24 = cmul(E16, E8), E32 = csqr(E16), E64 = csqr(E32);
    const cf c32 = cmul(coef, E32), c64 = cmul(coef, E64), c96 = cmul(c64, E32);
    const unsigned u1 = h2c(E8), u2 = h2c(E16), u3 = h2c(E24);
    const unsigned v[4] = {h2c(coef), h2c(c32), h2c(c64), h2c(c96)};
    #pragma unroll
    for (int q = 0; q < 4; ++q) { ROW(4 * q) = v[q]; ROW(4 * q + 1) = hmul(v[q], u1); ROW(4 * q + 2) = hmul(v[q], u2); ROW(4 * q + 3) = hmul(v[q], u3); }
    #pragma unroll
    for (int k = 0; k < 4; ++k) Bf[k] = tab_frag(trd, k);
  }
  hook(1);
  auto kmm = [&]() {
    f32x4 a = f32x4{0, 0, 0, 0};
    #pragma unroll
    for (int k = 0; k < 4; ++k) a = __builtin_amdgcn_mfma_f32_16x16x32_f16(tab_frag(trd, k), Bf[k], a, 0, 0, 0);
    return a * inv_scale;
  };
  const cf P2 = csqr(E1), P3 = cmul(P2, E1), P4 = csqr(P2), P6 = cmul(P4, P2), E132 = cmul(E128, P4);
  const cf E256 = csqr(E128), E384 = cmul(E256, E128), E512 = csqr(E256), E768 = cmul(E512, E256);
  const cf E1536 = cmul(E1024, E512), E2048 = csqr(E1024);
  const unsigned h1 = h2(E1), hp2 = h2(P2), hp3 = h2(P3), hp4 = h2(P4), hp6 = h2(P6), h128 = h2(E128), h132 = h2(E132);
  const unsigned h256 = h2(E256), h384 = h2(E384), h512 = h2(E512), h768 = h2(E768), h1024 = h2(E1024), h1536 = h2(E1536), h2048 = h2(E2048);
  asm volatile("" : "+v"(Bf[0]), "+v"(Bf[1]), "+v"(Bf[2]), "+v"(Bf[3]));
  if constexpr (TILES & 1) {
    ROW(0) = 0x00003c00u; ROW(1) = h1; ROW(2) = hp2; ROW(3) = hp3;
    ROW(4) = hp4; ROW(5) = hmul(hp4, h1); ROW(6) = hp6; ROW(7) = hmul(hp4, hp3);
    ROW(8) = h128; ROW(9) = hmul(h128, h1); ROW(10) = hmul(h128, hp2); ROW(11) = hmul(h128, hp3);
    ROW(12) = h132; ROW(13) = hmul(h132, h1); ROW(14) = hmul(h132, hp2); ROW(15) = hmul(h132, hp3);
    const f32x4 k = kmm();
    #pragma unroll
    for (int r = 0; r < 4; ++r) kp[2 * (128 * (gk >> 1) + 4 * (gk & 1) + 8 * i16 + r)] = k[r];
  }
  hook(2);
  if constexpr (TILES & 2) {
    ROW(0) = h256; ROW(1) = hmul(h256, hp2); ROW(2) = hmul(h256, hp4); ROW(3) = hmul(h256, hp6);
    ROW(4) = h384; ROW(5) = hmul(h384, hp2); ROW(6) = hmul(h384, hp4); ROW(7) = hmul(h384, hp6);
    ROW(8) = h512; ROW(9) = hmul(h512, hp4); ROW(10) = hmul(h512, h128); ROW(11) = hmul(h512, h132);
    ROW(12) = h768; ROW(13) = hmul(h768, hp4); ROW(14) = hmul(h768, h128); ROW(15) = hmul(h768, h132);
    const f32x4 k = kmm();
    const int p0 = gk < 2 ? 256 + 64 * gk + 4 * i16 : 384 + 64 * (gk - 2) + 2 * i16, st2 = gk < 2 ? 2 : 32;
    kp[2 * p0] = k[0]; kp[2 * (p0 + 1)] = k[1]; kp[2 * (p0 + st2)] = k[2]; kp[2 * (p0 + st2 + 1)] = k[3];
  }
  hook(3);
  if constexpr (TILES & 4) {
    ROW(0) = h1024; ROW(1) = hmul(h1024, h128); ROW(2) = hmul(h1024, h256); ROW(3) = hmul(h1024, h384);
    ROW(4) = h1536; ROW(5) = hmul(h1536, h128); ROW(6) = hmul(h1536, h256); ROW(7) = hmul(h1536, h384);
    ROW(8) = h2048;
    const f32x4 k = kmm();
    if (gk < 2) {
      #pragma unroll
      for (int r = 0; r < 4; ++r) kp[2 * (512 + 16 * (4 * gk + r) + i16)] = k[r];
    } else if (gk == 2) kp[2 * (640 + i16)] = k[0];
  }
  hook(4);
  #undef ROW
}

__global__ __launch_bounds__(512, 2) void fused_kernel(
    const u16* __restrict__ x, const u16* __restrict__ Are, const u16* __restrict__ Aim, const u16* __restrict__ Bre,
    const u16* __restrict__ Bim, const u16* __restrict__ Cre, const u16* __restrict__ Cim, const u16* __restrict__ logdt,
    const u16* __restrict__ Dskip, const u16* __restrict__ wg, const u16* __restrict__ wu, const u16* __restrict__ wd,
    const u16* __restrict__ gw, u16* __restrict__ out, char* __restrict__ ws
) {
  extern __shared__ __attribute__((aligned(1024))) char shm_[];
  lchar* shm = (lchar*)shm_;
  const int tid = threadIdx.x, wid = __builtin_amdgcn_readfirstlane(tid >> 6), lane = tid & 63;
  const int b = blockIdx.x, xcd = b & 7, bi = b >> 3;
  const int mt = (bi & 7) + 8 * (xcd >> 2), jc = (bi >> 3) + 4 * (xcd & 3);
  const int ks0 = ((bi & 7) + 2 * (bi >> 3)) & 7;
  const int mt64 = 4 * xcd + (bi & 3), nt = bi >> 2;
  const int kc = b >> 4, ct = b & 15;
  const int hidx = (xcd & 3) * 32 + bi, tb = 16 * (xcd >> 2) + (hidx & 15), cb = hidx >> 4;
  const rsrc_t rws = mk(ws, WS_DBG);
  if (wid == CODEPF_WAVE) {
    const u64 pc = __builtin_amdgcn_s_getpc() & ~(u64)255;
    const unsigned ofs = (unsigned)(CODEPF_LINES * bi + (lane & (CODEPF_LINES - 1))) * 128u;
    touch_line((const char*)pc + (ofs < CODE_BYTES ? ofs : CODE_BYTES - 128u), shm + L_DUMMY);
  }

  u64 tag_all;
  float sk_dsk = 0.f;
  bf16x8 Wr[28];
  const int nh = wid & 1, kq = wid >> 1;
  auto load_wfrags = [&](int lo, int hi) {
    const unsigned base = WS_WF + (unsigned)((2 * nt + nh) * 112 + 28 * kq) * 1024u;
    #pragma unroll
    for (int s = 0; s < 7; ++s) {
      #pragma unroll
      for (int q = 0; q < 4; ++q)
        if (4 * s + q >= lo && 4 * s + q < hi)
          Wr[4 * s + q] = as_frag(__builtin_amdgcn_raw_buffer_load_b128(rws, lane * 16, base + (4 * s + q) * 1024, SC1));
    }
  };

  if (wid >= 4) {
    const int s = wid & 3;
    const rsrc_t rg = mk(wg, D * F * 2), ru = mk(wu, D * F * 2), rx = mk(x, L * D * 2);
    unsigned vw[4], vx[4];
    #pragma unroll
    for (int q = 0; q < 4; ++q) {
      const int c = (4 * s + q) * 64 + lane;
      const int r = (c * 4370) >> 16, a = c - 15 * r;
      const int k = (r & ~12) | ((r & 4) << 1) | ((r & 8) >> 1);
      vw[q] = (a == 14 || r > 63) ? 0x7ffffff0u : (unsigned)(k * (F * 2) + jc * 224 + a * 16);
      const int t = 8 * (4 * s + q) + (lane >> 3);
      vx[q] = (mt * 128 + t) * (D * 2) + (((lane & 7) ^ ((t >> 1) & 7)) * 16);
    }
    auto piece = [&](int st, int i) {
      const int q = i / 3, kind = i % 3;
      const int ks = (st + ks0) & 7;
      lchar* base = shm + (st % 3) * STAGE;
      const unsigned sw = ks * 64 * F * 2, sx = ks * 128;
      if (kind == 0) blds16(rg, (q == 3 && s == 3) ? shm + L_DUMMY : base + (4 * s + q) * 1024, vw[q], sw);
      else if (kind == 1) blds16(ru, (q == 3 && s == 3) ? shm + L_DUMMY : base + UPOFF + (4 * s + q) * 1024, vw[q], sw);
      else blds16(rx, base + WREG + (4 * s + q) * 1024, vx[q], sx);
    };
    auto issue = [&](int st) {
      #pragma unroll
      for (int i = 0; i < 12; ++i) piece(st, i);
    };
    lchar* s1 = shm + L_JSCR + s * 4096;
    lchar* s2 = s1 + 2048;

    const int ch = s & 1, nhalf = s >> 1, chan = 2 * b + ch;
    #define WAITST(extra) WAITV(12 + (extra))
    #define PC(st, i) do { SB(); piece(st, i); SB(); } while (0)
    issue(0);
    {
      const rsrc_t rwd = mk(wd, F * D * 2);
      #pragma unroll
      for (int u = 0; u < 2; ++u)
        blds16(rwd, s2 + u * 1024, s + 4 * u < 7 ? (unsigned)((lane >> 2) * 1024 + (lane & 3) * 16) : 0x7ffffff0u, (unsigned)((112 * kc + 16 * (s + 4 * u)) * 1024 + ct * 64));
      #pragma unroll
      for (int u = 0; u < 2; ++u)
        blds16(rx, shm + L_AT + (2 * s + u) * 1024, (unsigned)((lane >> 3) * 1024 + (lane & 7) * 16), (unsigned)((64 * tb + 16 * s + 8 * u) * 1024 + 64 * cb * 2));
    }
    const u32x4 gwv = *(const u32x4*)(gw + lane * 8);
    const u32x4 xv0 = *(const u32x4*)(x + (8 * b + 2 * s) * D + lane * 8), xv1 = *(const u32x4*)(x + (8 * b + 2 * s + 1) * D + lane * 8);
    const u16 pDs = Dskip[chan];
    const u64 e0 = flag_load(ws, WS_EPOCH);
    SB();
    const int gk = lane >> 4, i16 = lane & 15;
    WAITV(0);
    const u64 tag = (((u64)__builtin_amdgcn_readfirstlane((unsigned)(e0 >> 32)) << 32) | __builtin_amdgcn_readfirstlane((unsigned)e0)) + 1;
    tag_all = tag;
    if (s == 0 && lane == 0) *(LAS u64*)(shm + L_SYNC + 128) = tag;
    const float dsk = bf2f(pDs);
    {
      float sm[2] = {0.f, 0.f};
      #pragma unroll
      for (int e = 0; e < 4; ++e) {
        const unsigned xa = xv0[e], xb = xv1[e], ga = gwv[e];
        sm[0] = __builtin_amdgcn_fdot2_f32_bf16(__builtin_bit_cast(bf16x2_t, xa), __builtin_bit_cast(bf16x2_t, ga), sm[0], false);
        sm[1] = __builtin_amdgcn_fdot2_f32_bf16(__builtin_bit_cast(bf16x2_t, xb), __builtin_bit_cast(bf16x2_t, ga), sm[1], false);
      }
      auto sw = __builtin_amdgcn_permlane32_swap(__float_as_uint(sm[0]), __float_as_uint(sm[1]), false, false);
      float t = __uint_as_float(sw[0]) + __uint_as_float(sw[1]);
      t += dpp_f<0x111>(t); t += dpp_f<0x112>(t); t += dpp_f<0x114>(t); t += dpp_f<0x118>(t);
      t += dpp_f<0x142>(t);
      const float gate2 = rbf(__builtin_amdgcn_rcpf(1.f + __builtin_amdgcn_exp2f(rbf(t) * -1.44269504f)));
      if ((lane & 31) == 31) __hip_atomic_store((GAS float*)(ws + WS_G) + 8 * b + 2 * s + (lane >> 5), gate2, __ATOMIC_RELAXED, __HIP_MEMORY_SCOPE_AGENT);
    }
    WAITL0();
    BAR();
    #define NEXT_B() do { WAITST(0); BAR(); SB(); } while (0)
    issue(1);
    SB();
    {
      const int h = lane >> 5, blk = (lane >> 4) & 1, q = (lane & 15) >> 2, p = lane & 3;
      s16x4 r[8];
      const unsigned a2 = (unsigned)(size_t)(s2 + (8 * h + q) * 64 + blk * 32 + p * 8), a1 = (unsigned)(size_t)(shm + L_AT + (8 * gk + q) * 128 + s * 32 + p * 8);
      asm volatile("ds_read_b64_tr_b16 %0, %8\n\tds_read_b64_tr_b16 %1, %8 offset:256\n\tds_read_b64_tr_b16 %2, %8 offset:1024\n\tds_read_b64_tr_b16 %3, %8 offset:1280\n\t"
                   "ds_read_b64_tr_b16 %4, %9\n\tds_read_b64_tr_b16 %5, %9 offset:512\n\tds_read_b64_tr_b16 %6, %9 offset:4096\n\tds_read_b64_tr_b16 %7, %9 offset:4608\n\ts_waitcnt lgkmcnt(0)"
                   : "=&v"(r[0]), "=&v"(r[1]), "=&v"(r[2]), "=&v"(r[3]), "=&v"(r[4]), "=&v"(r[5]), "=&v"(r[6]), "=&v"(r[7]) : "v"(a2), "v"(a1) : "memory");
      #pragma unroll
      for (int u = 0; u < 2; ++u)
        if (s + 4 * u < 7) st128(__builtin_bit_cast(u32x4, cat(r[2 * u], r[2 * u + 1])), rws, lane * 16 + WS_WF + (unsigned)(ct * 112 + 7 * kc + s + 4 * u) * 1024u);
      #pragma unroll
      for (int v = 0; v < 2; ++v)
        st128(__builtin_bit_cast(u32x4, cat(r[4 + 2 * v], r[5 + 2 * v])), rws,
            (unsigned)((16 * s + i16) * (L * 2) + (32 * v + 8 * gk) * 2) + WS_XT + (unsigned)(64 * cb * (L * 2) + 64 * tb * 2));
    }
    issue(2);
    NEXT_B();
    if (s == 0 && lane == 0) {
      flag_store(ws, WS_FX + (cb * 32 + tb) * 8, tag);
      flag_store(ws, WS_FW + (ct * 16 + kc) * 8, tag);
    }
    SB();
    u32x4 kc4[3][4];
    {
      lchar* kt = shm + L_KP + ch * KPCH;
      auto ent = [&](int p) { const f32x2 v = *(const LAS f32x2*)(kt + 8 * p); return v[0] + v[1]; };
      auto fin = [&](int it, const unsigned* P, const unsigned* Q) {
        kc4[it][0] = u32x4{P[0], P[1], P[2], P[3]}; kc4[it][1] = u32x4{Q[0], Q[1], Q[2], Q[3]};
        kc4[it][2] = u32x4{P[1], P[2], P[3], P[4]}; kc4[it][3] = u32x4{Q[1], Q[2], Q[3], Q[4]};
        #pragma unroll
        for (int c = 0; c < 4; ++c) asm volatile("" : "+v"(kc4[it][c]));
      };
      if (nhalf == 0) {
        PC(3, 0);
        const float A0 = ent(640 - lane), B0_ = ent(639 - lane), A1 = ent(576 - lane), B1_ = ent(575 - lane);
        const int q2 = 128 + lane;
        const float A2 = ent(768 - 2 * q2), B2 = ent(767 - 2 * q2), C2 = ent(766 - 2 * q2), D2 = ent(q2 == 191 ? 383 : 766 - 2 * q2);
        PC(3, 1); PC(3, 2);
        const bool even = !(lane & 1);
        #pragma unroll
        for (int it = 0; it < 2; ++it) {
          const float A = it ? A1 : A0, B = it ? B1_ : B0_;
          const unsigned AA = pk_bf16_asm(A, A), AB = pk_bf16_asm(A, B), BB = pk_bf16_asm(B, B);
          const unsigned P[5] = {AA, AA, even ? AB : BB, BB, BB}, Q[5] = {AA, even ? AA : AB, BB, BB, BB};
          fin(it, P, Q);
          PC(3, 3 + it);
        }
        {
          const unsigned AA = pk_bf16_asm(A2, A2), AB = pk_bf16_asm(A2, B2), BB = pk_bf16_asm(B2, B2), BC = pk_bf16_asm(B2, C2), CC = pk_bf16_asm(C2, C2), CD = pk_bf16_asm(C2, D2);
          const unsigned P[5] = {AA, AB, BB, CC, CC}, Q[5] = {AA, BB, BC, CC, CD};
          fin(2, P, Q);
        }
        #pragma unroll
        for (int i = 5; i < 12; ++i) PC(3, i);
      } else {
        PC(3, 0);
        const int q = 192 + lane, s0 = 2048 - 8 * q;
        const bool lo = lane < 32;
        const int pb = lo ? 128 + s0 / 2 - 6 : s0 - 10;
        float T[12];
        #pragma unroll
        for (int k = 0; k < 6; ++k) { const f32x4 v = *(const LAS f32x4*)(kt + 8 * pb + 16 * k); T[2 * k] = v[0] + v[1]; T[2 * k + 1] = v[2] + v[3]; }
        PC(3, 1); PC(3, 2);
        float e[11];
        const int il[11] = {6, 6, 5, 4, 4, 4, 3, 2, 2, 2, 1};
        #pragma unroll
        for (int j = 0; j < 11; ++j) e[j] = lo ? T[il[j]] : T[10 - j];
        if (q == 223) { e[9] = T[1]; e[10] = T[0]; }
        if (q == 255) { e[9] = 0.f; e[10] = 0.f; }
        PC(3, 3); PC(3, 4);
        unsigned P[5], Q[5];
        #pragma unroll
        for (int j = 0; j < 5; ++j) { P[j] = pk_bf16_asm(e[2 * j], e[2 * j + 1]); Q[j] = pk_bf16_asm(e[2 * j + 1], e[2 * j + 2]); }
        fin(0, P, Q);
        kc4[1][0] = kc4[1][1] = kc4[1][2] = kc4[1][3] = kc4[2][0] = kc4[2][1] = kc4[2][2] = kc4[2][3] = u32x4{0, 0, 0, 0};
        #pragma unroll
        for (int i = 5; i < 12; ++i) PC(3, i);
      }
    }
    float kz = *(const LAS float*)(shm + L_KP + ch * KPCH) + *(const LAS float*)(shm + L_KP + ch * KPCH + 4);
    asm volatile("" : "+v"(kz));
    NEXT_B();
    issue(4);
    NEXT_B();
    issue(5);
    NEXT_B();
    const u64 pollW = flag_load(ws, WS_FW + (64 * s + lane) * 8);
    const u64 pollX = flag_load(ws, WS_FX + (b >> 5) * 32 * 8 + (lane & 31) * 8);
    SB();
    issue(6);
    NEXT_B();
    issue(7);
    const char* wfg = ws + WS_WF + (size_t)s * (4 * 112 * 1024) + (size_t)bi * (112 * 128) + lane * 128;
    WAITV(12);
    BAR();
    SB();
    if (!__all(pollW == tag)) wait_flags(ws, WS_FW + 64 * s * 8, 64, tag, lane);
    if (!__all(pollX == tag)) wait_flags(ws, WS_FX + (b >> 5) * 32 * 8, 32, tag, lane);
    lchar* uimg = shm + L_U + ch * UIMG;
    #pragma unroll
    for (int q = 0; q < 2; ++q) {
      const int P = (2 * nhalf + q) * 64 + lane;
      const int c = P >> 2, g = (P & 3) ^ (2 * ((c >> 2) & 1));
      blds16<SC1>(rws, uimg + 1024 + (2 * nhalf + q) * 1024, (unsigned)(c * 64 + g * 16), WS_XT + (unsigned)chan * (L * 2));
    }
    SB();
    touch_line_sc1(wfg, shm + L_DUMMY); touch_line_sc1(wfg + (lane < 48 ? 64 * 128 : 0), shm + L_DUMMY);
    SB();
    if (nhalf == 0) *(LAS u32x4*)(uimg + lane * 16) = u32x4{0, 0, 0, 0};
    {
      lchar* kc = shm + L_KC + ch * 4 * KCOPY;
      if (nhalf == 0) {
        #pragma unroll
        for (int it = 0; it < 3; ++it)
          #pragma unroll
          for (int c = 0; c < 4; ++c) *(LAS u32x4*)(kc + c * KCOPY + (it * 64 + lane) * 16) = kc4[it][c];
      } else {
        #pragma unroll
        for (int c = 0; c < 4; ++c) *(LAS u32x4*)(kc + c * KCOPY + (192 + lane) * 16) = kc4[0][c];
      }
      if (nhalf == 1 && lane < 16) {
        const int cp = lane >> 2, q = 256 + (lane & 3);
        u32x4 v = {0, 0, 0, 0};
        if (cp == 0 && q == 256) v[0] = pk_bf16(kz, 0.f);
        *(LAS u32x4*)(kc + cp * KCOPY + q * 16) = v;
      }
    }
    WAITV(2); WAITL0(); BAR();
    {
      f32x4 ya[2][4];
      #pragma unroll
      for (int bb = 0; bb < 2; ++bb)
        #pragma unroll
        for (int ab = 0; ab < 4; ++ab) ya[bb][ab] = f32x4{0, 0, 0, 0};
      const int x0 = 2048 - i16 + 8 * gk;
      lchar* fb = shm + L_KC + ch * 4 * KCOPY + (x0 & 3) * KCOPY + (x0 & ~3) * 2 - 3 * 1024 - 32 - 512 * nhalf;
      bf16x8 Ff[2][4][2], Gf[2][4];
      auto cload = [&](int buf, int dd) {
        lchar* f = fb - 64 * dd;
        const int c = i16 - (8 * nhalf + dd) + 16;
        lchar* gp = uimg + c * 64 + ((gk ^ (2 * ((c >> 2) & 1))) * 16);
        #pragma unroll
        for (int i = 0; i < 4; ++i)
          #pragma unroll
          for (int bb = 0; bb < 2; ++bb) {
            lchar* a = f + 1024 * (3 - i) + 32 * (1 - bb);
            const s16x4 lo = *(const volatile LAS s16x4*)a, hi = *(const volatile LAS s16x4*)(a + 8);
            Ff[buf][i][bb] = cat(lo, hi);
          }
        #pragma unroll
        for (int k = 0; k < 4; ++k) Gf[buf][k] = lds_b128(gp + 1024 * k);
      };
      cload(0, 0);
      SB();
      load_wfrags(0, 14);
      SB();
      lds_wait(shm, 9 + s, 1);
      #pragma unroll
      for (int dd = 0; dd < 8; ++dd) {
        if (dd < 7) cload((dd + 1) & 1, dd + 1);
        #pragma unroll
        for (int ab = 0; ab < 4; ++ab)
          #pragma unroll
          for (int i = 0; i <= ab; ++i)
            #pragma unroll
            for (int bb = 0; bb < 2; ++bb) ya[bb][ab] = __builtin_amdgcn_mfma_f32_16x16x32_bf16(Ff[dd & 1][i][bb], Gf[dd & 1][ab - i], ya[bb][ab], 0, 0, 0);
        if (dd < 7) {
          #pragma unroll
          for (int q = 0; q < 20; ++q) { __builtin_amdgcn_sched_group_barrier(0x8, 1, 0); __builtin_amdgcn_sched_group_barrier(0x100, 1, 0); }
        }
        SB();
      }
      lchar* yp = shm + L_Y + (ch * 2 + nhalf) * 8192;
      #pragma unroll
      for (int bb = 0; bb < 2; ++bb)
        #pragma unroll
        for (int ab = 0; ab < 4; ++ab)
          *(LAS f32x4*)(yp + (16 * ab + i16) * 128 + (((4 * bb + gk) ^ (i16 & 7)) * 16)) = ya[bb][ab];
    }
    sk_dsk = dsk;
    } else {
    if (wid == 0 && lane < 16) ((LAS unsigned*)(shm + L_SYNC))[lane] = 0;
    const int chan = 2 * b + (wid & 1), pi = chan * NS + 64 * (wid >> 1) + lane;
    const KPar kpar = {Are[pi], Aim[pi], Bre[pi], Bim[pi], Cre[pi], Cim[pi], logdt[chan]};
    SB();
    const int wm = wid & 1, wn = wid >> 1;
    const int gk = lane >> 4, qq = (lane >> 2) & 3, pp = lane & 3;
    const int kk = 8 * gk + qq, pik = (kk & ~12) | ((kk & 4) << 1) | ((kk & 8) >> 1);
    const unsigned abase = (pp >> 1) * UPOFF + (15 * pik + 7 * wn) * 16 + 8 * (pp & 1);
    const int tok = lane & 15;
    const unsigned xb0 = WREG + (64 * wm + tok) * 128 + (((0 + gk) ^ ((tok >> 1) & 7)) * 16);
    const unsigned xb1 = WREG + (64 * wm + tok) * 128 + (((4 + gk) ^ ((tok >> 1) & 7)) * 16);
    unsigned abase_ = abase, xb0_ = xb0, xb1_ = xb1;
    f32x4 acc[7][4];
    asm volatile("" : "+v"(abase_), "+v"(xb0_), "+v"(xb1_));
    kgen<7>(kpar, shm + L_GTAB + wid * KTAB, (LAS float*)(shm + L_KP + (wid & 1) * KPCH) + (wid >> 1), lane, [&](int i) {
    });
    bf16x8 A0[7], B0[4], A1[7], B1[4];
    auto rd = [&](bf16x8* A, bf16x8* B, unsigned sb, int ks) {
      lchar* pa = shm + (abase_ + sb);
      lchar* px = shm + ((ks ? xb1_ : xb0_) + sb);
      #pragma unroll
      for (int a = 0; a < 7; ++a) A[a] = cat(tr_read(pa + a * 16 + ks * 7680), tr_read(pa + a * 16 + ks * 7680 + 1920));
      #pragma unroll
      for (int t = 0; t < 4; ++t) B[t] = lds_b128(px + t * 2048);
    };
    auto mm = [&](bf16x8* A, bf16x8* B) {
      #pragma unroll
      for (int a = 0; a < 7; ++a)
        #pragma unroll
        for (int t = 0; t < 4; ++t) acc[a][t] = __builtin_amdgcn_mfma_f32_16x16x32_bf16(A[a], B[t], acc[a][t], 0, 0, 0);
    };
    auto sched = [&]() {
      #pragma unroll
      for (int q = 0; q < 18; ++q) { __builtin_amdgcn_sched_group_barrier(0x8, 1, 0); __builtin_amdgcn_sched_group_barrier(0x100, 1, 0); }
      __builtin_amdgcn_sched_group_barrier(0x8, 10, 0);
    };
    WAITL0();
    BAR();
    __builtin_amdgcn_s_setprio(1);
    rd(A0, B0, 0, 0);
    SB();
    rd(A1, B1, 0, 1);
    #pragma unroll
    for (int a = 0; a < 7; ++a)
      #pragma unroll
      for (int t = 0; t < 4; ++t) acc[a][t] = __builtin_amdgcn_mfma_f32_16x16x32_bf16(A0[a], B0[t], f32x4{0, 0, 0, 0}, 0, 0, 0);
    sched();
    SB();
    #pragma unroll 1
    for (int st = 0; st < 7; ++st) {
      const unsigned sn = (unsigned)((st + 1) % 3) * STAGE;
      WAITL0();
      BAR();
      SB();
      rd(A0, B0, sn, 0);
      mm(A1, B1);
      sched();
      SB();
      rd(A1, B1, sn, 1);
      mm(A0, B0);
      sched();
      SB();
    }
    mm(A1, B1);
    SB();
    __builtin_amdgcn_s_setprio(0);
    const u64 tag = *(const LAS u64*)(shm + L_SYNC + 128);
    const int Y = lane >> 5, g2 = (lane >> 4) & 1;
    #pragma unroll
    for (int p = 0; p < 2; ++p) {
      const int tile32 = 4 * mt + 2 * wm + p;
      #pragma unroll
      for (int a = 0; a < 7; ++a) {
        float h[4];
        #pragma unroll
        for (int r = 0; r < 4; r += 2) {
          auto s0 = __builtin_amdgcn_permlane32_swap(__float_as_uint(acc[a][2 * p][r]), __float_as_uint(acc[a][2 * p + 1][r]), false, false);
          auto s1 = __builtin_amdgcn_permlane32_swap(__float_as_uint(acc[a][2 * p][r + 1]), __float_as_uint(acc[a][2 * p + 1][r + 1]), false, false);
          const f32x2 gv = f32x2{__uint_as_float(s0[0]), __uint_as_float(s1[0])} * -1.44269504f;
          const f32x2 d = f32x2{__builtin_amdgcn_exp2f(gv[0]), __builtin_amdgcn_exp2f(gv[1])} + 1.f;
          const f32x2 hv = f32x2{__uint_as_float(s0[1]), __uint_as_float(s1[1])} * f32x2{__builtin_amdgcn_rcpf(d[0]), __builtin_amdgcn_rcpf(d[1])};
          h[r] = hv[0]; h[r + 1] = hv[1];
        }
        const int ag = 7 * wn + a, ks = 7 * jc + (ag >> 1), hh = ag & 1;
        const u32x2 v = {pk_bf16(h[0], h[1]), pk_bf16(h[2], h[3])};
        __builtin_amdgcn_raw_buffer_store_b64(v, rws, (unsigned)((32 * hh + 16 * Y + tok) * 16 + 8 * g2), WS_HF + (unsigned)(tile32 * 112 + ks) * 1024u, SC1);
        if (p == 1 && a == 6 && lane == 0) *(volatile LAS unsigned*)(shm + L_SYNC + (9 + wid) * 4) = 1u;
      }
    }
    WAITV(0);
    if (lds_arrive(shm, 1, lane) == 3 && lane == 0) flag_store(ws, WS_FH + (mt * 16 + jc) * 8, tag);
    SB();
    load_wfrags(0, 28);
    SB();
    if (wid == 0) wait_flags(ws, WS_FH + (mt64 >> 1) * 16 * 8, 16, tag, lane);
    tag_all = tag;
  }
  const u64 tag = tag_all;
  WAITL0();
  BAR();
  {
    auto issue2k = [&](int st, int q, int k) {
      blds16<SC1>(rws, shm + L_RING + (st % 3) * 32768 + k * 8192 + nh * 4096 + q * 1024, (unsigned)(lane * 16), WS_HF + (unsigned)((2 * mt64 + nh) * 112 + 28 * k + 4 * st + q) * 1024u);
    };
    auto issue2q = [&](int st, int q) { issue2k(st, q, kq); };
    const int tokl = 16 * (wid & 3) + (lane & 15), dh = wid >> 2, G = lane >> 4;
    if (wid < 4) {
      #pragma unroll
      for (int q = 0; q < 4; ++q) { issue2k(0, q, kq); issue2k(0, q, kq + 2); }
    }
    if (wid >= 4) {
      const int s = wid & 3, ch = s & 1, nhalf = s >> 1, chan = 2 * b + ch;
      lchar* y0 = shm + L_Y + ch * 2 * 8192;
      lchar* uimg = shm + L_U + ch * UIMG;
      #pragma unroll
      for (int it = 0; it < 2; ++it) {
        const int q = nhalf * 128 + it * 64 + lane;
        const int a = q >> 2, c0 = 2 * (q & 3);
        f32x4 p0 = *(const LAS f32x4*)(y0 + a * 128 + ((c0 ^ (a & 7)) * 16)) + *(const LAS f32x4*)(y0 + 8192 + a * 128 + ((c0 ^ (a & 7)) * 16));
        f32x4 p1 = *(const LAS f32x4*)(y0 + a * 128 + (((c0 + 1) ^ (a & 7)) * 16)) + *(const LAS f32x4*)(y0 + 8192 + a * 128 + (((c0 + 1) ^ (a & 7)) * 16));
        const u32x4 uv = *(const LAS u32x4*)(uimg + (a + 16) * 64 + (((q & 3) ^ (2 * ((a >> 2) & 1))) * 16));
        u32x4 o;
        #pragma unroll
        for (int e = 0; e < 4; ++e) {
          const float ya_ = e < 2 ? p0[2 * e] : p1[2 * e - 4], yb_ = e < 2 ? p0[2 * e + 1] : p1[2 * e - 3];
          const unsigned yr = pk_bf16(ya_, yb_), sk = pk_bf16(bf_lo(uv[e]) * sk_dsk, bf_hi(uv[e]) * sk_dsk);
          unsigned oe = pk_bf16(bf_lo(yr) + bf_lo(sk), bf_hi(yr) + bf_hi(sk));
          o[e] = oe;
        }
        st128(o, rws, (unsigned)(q * 16) + WS_AT + (unsigned)chan * (L * 2));
      }
      SB();
    }
    if (wid >= 4) {
      #pragma unroll
      for (int q = 0; q < 4; ++q) { issue2k(1, q, kq); issue2k(1, q, kq - 2); }
    }
    SB();
    f32x16 acc2[2];
    #pragma unroll
    for (int t = 0; t < 2; ++t)
      #pragma unroll
      for (int e = 0; e < 16; ++e) acc2[t][e] = 0.f;
    bf16x8 hf[2][4][2];
    auto rd2 = [&](int st) {
      lchar* src = shm + L_RING + (st % 3) * 32768 + kq * 8192 + lane * 16;
      #pragma unroll
      for (int q = 0; q < 4; ++q)
        #pragma unroll
        for (int t = 0; t < 2; ++t) hf[st & 1][q][t] = lds_b128(src + t * 4096 + q * 1024);
    };
    if (wid < 4) WAITV(0);
    BAR();
    if (wid < 4) {
      #pragma unroll
      for (int q = 0; q < 4; ++q) { issue2k(2, q, kq); issue2k(2, q, kq + 2); }
    }
    rd2(0);
    SB();
    u64 pollA = 0;
    float gate = 0.f;
    const unsigned pollo = lane < 32 ? WS_FA + (32 * nt + lane) * 8 : WS_FG + (8 * mt64 + (lane & 7)) * 8;
    #pragma unroll
    for (int st = 0; st < 7; ++st) {
      if (st < 6) {
        if (st == 0) { if (wid >= 4) WAITV(0); }
        else if (st == 1) { if (wid < 4) WAITV(4); }
        else if (st == 2) { if (wid >= 4) WAITV(12); else WAITV(4); }
        else if (st < 5) WAITV(4);
        else WAITV(1);
        WAITL0();
        BAR();
        SB();
        rd2(st + 1);
        SB();
      }
      if (st == 0 && wid == 4 && lane == 0) { flag_store(ws, WS_FA + b * 8, tag); flag_store(ws, WS_FG + b * 8, tag); }
      if (st == 4) { pollA = flag_load(ws, pollo); SB(); }
      if (st == 5) {
        if (!__all(pollA == tag)) wait_flags_at(ws, pollo, tag, lane);
        blds16<SC1>(rws, shm + L_AT + wid * 1024, (unsigned)((lane >> 3) * (L * 2) + (lane & 7) * 16), WS_AT + (unsigned)((64 * nt + 8 * wid) * (L * 2) + 64 * mt64 * 2));
        gate = __uint_as_float(__builtin_amdgcn_raw_buffer_load_b32(rws, (unsigned)(tokl * 4), WS_G + (unsigned)(64 * mt64 * 4), SC1));
        SB();
      }
      #pragma unroll
      for (int q = 0; q < 4; ++q) {
        #pragma unroll
        for (int t = 0; t < 2; ++t) acc2[t] = __builtin_amdgcn_mfma_f32_32x32x16_bf16(Wr[4 * st + q], hf[st & 1][q][t], acc2[t], 0, 0, 0);
        if (st + 3 < 7) { SB(); issue2q(st + 3, q); SB(); }
        if (st < 2 && wid >= 4) { if (st == 0 || q < 3) { load_wfrags(14 + 8 * st + 2 * q, 16 + 8 * st + 2 * q); SB(); } }
      }
      SB();
    }
    {
      lchar* pw = shm + (kq == 0 ? 0 : 65536 + (kq - 1) * PKQ) + (lane & 31) * PSTR + (32 * nh + 4 * (lane >> 5)) * 4;
      #pragma unroll
      for (int t = 0; t < 2; ++t) {
        #pragma unroll
        for (int q = 0; q < 4; ++q)
          *(LAS f32x4*)(pw + t * 32 * PSTR + q * 32) = f32x4{acc2[t][4 * q], acc2[t][4 * q + 1], acc2[t][4 * q + 2], acc2[t][4 * q + 3]};
      }
    }
    WAITV(0);
    WAITL0();
    BAR();
    {
      lchar* pr = shm + tokl * PSTR + (32 * dh + 8 * G) * 4;
      f32x4 s0 = f32x4{0, 0, 0, 0}, s1 = f32x4{0, 0, 0, 0};
      #pragma unroll
      for (int k = 0; k < 4; ++k) { s0 += *(const LAS f32x4*)(pr + pk_off(k)); s1 += *(const LAS f32x4*)(pr + pk_off(k) + 16); }
      const int q = (lane & 15) >> 2, p = lane & 3;
      lchar* aa = shm + L_AT + (32 * dh + 8 * G + q) * 128 + (16 * (wid & 3) + 4 * p) * 2;
      const s16x4 a0 = tr_read(aa), a1 = tr_read(aa + 512);
      float og = rbf(1.f - gate);
      u32x4 o;
      #pragma unroll
      for (int e = 0; e < 4; ++e) {
        const float b0 = e < 2 ? s0[2 * e] : s1[2 * e - 4], b1 = e < 2 ? s0[2 * e + 1] : s1[2 * e - 3];
        const float av0 = bf2f((u16)(e < 2 ? a0[2 * e] : a1[2 * e - 4])), av1 = bf2f((u16)(e < 2 ? a0[2 * e + 1] : a1[2 * e - 3]));
        const unsigned bb = pk_bf16(b0, b1), t1 = pk_bf16(gate * av0, gate * av1), t2 = pk_bf16(og * bf_lo(bb), og * bf_hi(bb));
        unsigned oe = pk_bf16(bf_lo(t1) + bf_lo(t2), bf_hi(t1) + bf_hi(t2));
        o[e] = oe;
      }
      *(u32x4*)(out + (64 * mt64 + tokl) * D + 64 * nt + 32 * dh + 8 * G) = o;
    }
  }
  if (b == 0 && wid == 0) {
    #pragma unroll
    for (int q = 0; q < 4; ++q) wait_flags(ws, WS_FG + q * 64 * 8, 64, tag, lane);
    if (lane == 0) flag_store(ws, WS_EPOCH, tag);
  }
}

extern "C" void kernel_launch(void* const* d_in, const int* in_sizes, int n_in, void* d_out, int out_size, void* d_ws, size_t ws_size,
                              hipStream_t stream) {
  (void)in_sizes; (void)n_in; (void)out_size; (void)ws_size;
  hipFuncSetAttribute((const void*)fused_kernel, hipFuncAttributeMaxDynamicSharedMemorySize, LDS_BYTES);
  fused_kernel<<<256, 512, LDS_BYTES, stream>>>(
      (const u16*)d_in[0], (const u16*)d_in[1], (const u16*)d_in[2], (const u16*)d_in[3], (const u16*)d_in[4], (const u16*)d_in[5],
      (const u16*)d_in[6], (const u16*)d_in[7], (const u16*)d_in[8], (const u16*)d_in[10], (const u16*)d_in[11], (const u16*)d_in[12],
      (const u16*)d_in[13], (u16*)d_out, (char*)d_ws
  );
}
```
